```python
import torch

def _softmax(x, axis=-1):
    return torch.softmax(x, dim=axis)
def _gelu(x):
    return torch.nn.functional.gelu(x, approximate="tanh")
def _silu(x):
    return torch.nn.functional.silu(x)
def _sigmoid(x):
    return torch.sigmoid(x)
def _layernorm(x, axis=-1, eps=1e-5):
    m = x.mean(dim=axis, keepdim=True)
    v = x.var(dim=axis, keepdim=True, unbiased=False)
    return (x - m) / torch.sqrt(v + eps)
def _scatter_add(tgt, idx, src, axis=0):
    return tgt.clone().index_add_(axis, idx, src)
def _segment_sum(src, seg, n):
    out = torch.zeros((n,) + src.shape[1:], dtype=src.dtype)
    return out.index_add_(0, seg, src)
def _pad(x, pw):
    flat = tuple(v for pair in reversed(pw) for v in pair)
    return torch.nn.functional.pad(x, flat)

kernel_name = 'standalone__glu_gated_geglu__prompt'

def setup_inputs():

    _w = (lambda a: (lambda t: t.to(torch.bfloat16) if t.is_floating_point() else t)(torch.as_tensor(a)))
    B, S, Dm, Df = 1, 2048, 640, 1728
    return {
        "x": _w((lambda *s: torch.randn(*s, dtype=torch.bfloat16) * 0.05)(B, S, Dm)),
        "w_gate": _w((lambda *s: torch.randn(*s, dtype=torch.bfloat16) * 0.05)(Dm, Df) * (Dm ** -0.5)),
        "w_up": _w((lambda *s: torch.randn(*s, dtype=torch.bfloat16) * 0.05)(Dm, Df) * (Dm ** -0.5)),
        "w_down": _w((lambda *s: torch.randn(*s, dtype=torch.bfloat16) * 0.05)(Df, Dm) * (Df ** -0.5)),
    }

def reference(x, w_gate, w_up, w_down):
    g = torch.einsum("bsd,df->bsf", x, w_gate)
    u = torch.einsum("bsd,df->bsf", x, w_up)
    h = _gelu(g) * u
    return torch.einsum("bsf,fd->bsd", h, w_down)
```

```cpp
#include <hip/hip_runtime.h>
#include <hip/hip_bf16.h>

typedef unsigned short u16;
typedef short bf16x8 __attribute__((ext_vector_type(8)));
typedef float f32x4 __attribute__((ext_vector_type(4)));

constexpr int M = 2048, DM = 640, DF = 1728;

__device__ __forceinline__ float bf2f(u16 v) { return __uint_as_float(((unsigned)v) << 16); }
__device__ __forceinline__ u16 f2bf(float f) { __hip_bfloat16 b = __float2bfloat16(f); return *reinterpret_cast<u16*>(&b); }

__device__ __forceinline__ float gelu_tanh(float g) {
    const float k0 = -2.0f * 0.7978845608028654f * 1.4426950408889634f;
    const float k1 = k0 * 0.044715f;
    float t = g * (k0 + k1 * g * g);
    float e = __builtin_amdgcn_exp2f(t);
    return g * __builtin_amdgcn_rcpf(1.0f + e);
}

template <bool GLU>
__global__ __launch_bounds__(256) void gemm_simple(const u16* __restrict__ A, const u16* __restrict__ B0, const u16* __restrict__ B1,
                                                   u16* __restrict__ C, int K, int N) {
    __shared__ __attribute__((aligned(16))) u16 As[64][40];
    __shared__ __attribute__((aligned(16))) u16 Bs[2][64][40];
    const int tid = threadIdx.x, lane = tid & 63, wid = tid >> 6;
    const int m0 = blockIdx.y * 64, n0 = blockIdx.x * 64;
    const int fr = lane & 15, fq = lane >> 4;
    f32x4 acc[2][4];
    for (int i = 0; i < 2; ++i) for (int j = 0; j < 4; ++j) acc[i][j] = (f32x4){0, 0, 0, 0};
    for (int k0 = 0; k0 < K; k0 += 32) {
        {
            int r = tid >> 2, c = (tid & 3) * 8;
            *(uint4*)&As[r][c] = *(const uint4*)&A[(size_t)(m0 + r) * K + k0 + c];
            int kr = tid >> 3, nc = (tid & 7) * 8;
            uint4 v = *(const uint4*)&B0[(size_t)(k0 + kr) * N + n0 + nc];
            const u16* p = (const u16*)&v;
            for (int j = 0; j < 8; ++j) Bs[0][nc + j][kr] = p[j];
            if (GLU) {
                uint4 w = *(const uint4*)&B1[(size_t)(k0 + kr) * N + n0 + nc];
                const u16* q = (const u16*)&w;
                for (int j = 0; j < 8; ++j) Bs[1][nc + j][kr] = q[j];
            }
        }
        __syncthreads();
        bf16x8 a = *(const bf16x8*)&As[wid * 16 + fr][fq * 8];
        for (int n = 0; n < 4; ++n) {
            bf16x8 b = *(const bf16x8*)&Bs[0][n * 16 + fr][fq * 8];
            acc[0][n] = __builtin_amdgcn_mfma_f32_16x16x32_bf16(a, b, acc[0][n], 0, 0, 0);
            if (GLU) {
                bf16x8 b1 = *(const bf16x8*)&Bs[1][n * 16 + fr][fq * 8];
                acc[1][n] = __builtin_amdgcn_mfma_f32_16x16x32_bf16(a, b1, acc[1][n], 0, 0, 0);
            }
        }
        __syncthreads();
    }
    for (int n = 0; n < 4; ++n)
        for (int j = 0; j < 4; ++j) {
            float v = acc[0][n][j];
            if (GLU) v = gelu_tanh(v) * acc[1][n][j];
            C[(size_t)(m0 + wid * 16 + fq * 4 + j) * N + n0 + n * 16 + fr] = f2bf(v);
        }
}

extern "C" void kernel_launch(void* const* d_in, const int* in_sizes, int n_in,
                              void* d_out, int out_size, void* d_ws, size_t ws_size,
                              hipStream_t stream) {
    const u16* x = (const u16*)d_in[0];
    const u16* wg = (const u16*)d_in[1];
    const u16* wu = (const u16*)d_in[2];
    const u16* wd = (const u16*)d_in[3];
    u16* h = (u16*)d_ws;
    u16* out = (u16*)d_out;
    gemm_simple<true><<<dim3(DF / 64, M / 64), 256, 0, stream>>>(x, wg, wu, h, DM, DF);
    gemm_simple<false><<<dim3(DM / 64, M / 64), 256, 0, stream>>>(h, wd, wd, out, DF, DM);
}
```

```cpp
#include <hip/hip_runtime.h>
#include <hip/hip_bf16.h>

typedef unsigned short u16;
typedef short bf16x8 __attribute__((ext_vector_type(8)));
typedef float f32x4 __attribute__((ext_vector_type(4)));

constexpr int M = 2048, DM = 640, DF = 1728;

__device__ __forceinline__ float bf2f(u16 v) { return __uint_as_float(((unsigned)v) << 16); }
__device__ __forceinline__ u16 f2bf(float f) { __hip_bfloat16 b = __float2bfloat16(f); return *reinterpret_cast<u16*>(&b); }

__device__ __forceinline__ float gelu_tanh(float g) {
    const float k0 = -2.0f * 0.7978845608028654f * 1.4426950408889634f;
    const float k1 = k0 * 0.044715f;
    float t = g * (k0 + k1 * g * g);
    float e = __builtin_amdgcn_exp2f(t);
    return g * __builtin_amdgcn_rcpf(1.0f + e);
}

template <bool GLU>
__global__ __launch_bounds__(256) void gemm_simple(const u16* __restrict__ A, const u16* __restrict__ B0, const u16* __restrict__ B1,
                                                   u16* __restrict__ C, int K, int N) {
    __shared__ __attribute__((aligned(16))) u16 As[64][40];
    __shared__ __attribute__((aligned(16))) u16 Bs[2][64][40];
    const int tid = threadIdx.x, lane = tid & 63, wid = tid >> 6;
    const int m0 = blockIdx.y * 64, n0 = blockIdx.x * 64;
    const int fr = lane & 15, fq = lane >> 4;
    f32x4 acc[2][4];
    for (int i = 0; i < 2; ++i) for (int j = 0; j < 4; ++j) acc[i][j] = (f32x4){0, 0, 0, 0};
    for (int k0 = 0; k0 < K; k0 += 32) {
        {
            int r = tid >> 2, c = (tid & 3) * 8;
            *(uint4*)&As[r][c] = *(const uint4*)&A[(size_t)(m0 + r) * K + k0 + c];
            int kr = tid >> 3, nc = (tid & 7) * 8;
            uint4 v = *(const uint4*)&B0[(size_t)(k0 + kr) * N + n0 + nc];
            const u16* p = (const u16*)&v;
            for (int j = 0; j < 8; ++j) Bs[0][nc + j][kr] = p[j];
            if (GLU) {
                uint4 w = *(const uint4*)&B1[(size_t)(k0 + kr) * N + n0 + nc];
                const u16* q = (const u16*)&w;
                for (int j = 0; j < 8; ++j) Bs[1][nc + j][kr] = q[j];
            }
        }
        __syncthreads();
        bf16x8 a = *(const bf16x8*)&As[wid * 16 + fr][fq * 8];
        for (int n = 0; n < 4; ++n) {
            bf16x8 b = *(const bf16x8*)&Bs[0][n * 16 + fr][fq * 8];
            acc[0][n] = __builtin_amdgcn_mfma_f32_16x16x32_bf16(a, b, acc[0][n], 0, 0, 0);
            if (GLU) {
                bf16x8 b1 = *(const bf16x8*)&Bs[1][n * 16 + fr][fq * 8];
                acc[1][n] = __builtin_amdgcn_mfma_f32_16x16x32_bf16(a, b1, acc[1][n], 0, 0, 0);
            }
        }
        __syncthreads();
    }
    for (int n = 0; n < 4; ++n)
        for (int j = 0; j < 4; ++j) {
            float v = acc[0][n][j];
            if (GLU) v = gelu_tanh(v) * acc[1][n][j];
            C[(size_t)(m0 + wid * 16 + fq * 4 + j) * N + n0 + n * 16 + fr] = f2bf(v);
        }
}


__global__ void probe(const char* p, int stride, int n, unsigned* sink) {
    unsigned v = 0;
    const char* a = p;
    for (int i = 0; i < n; ++i) {
        unsigned t;
        asm volatile("global_load_dword %0, %1, off\n\ts_waitcnt vmcnt(0)" : "=v"(t) : "v"(a) : "memory");
        v += t;
        a = a + stride + (t & 0u) * 4;
        asm volatile("" : "+v"(a));
    }
    if (v == 0x13579bdf) sink[0] = v;
}
extern "C" void kernel_launch(void* const* d_in, const int* in_sizes, int n_in,
                              void* d_out, int out_size, void* d_ws, size_t ws_size,
                              hipStream_t stream) {
    const u16* x = (const u16*)d_in[0];
    const u16* wg = (const u16*)d_in[1];
    const u16* wu = (const u16*)d_in[2];
    const u16* wd = (const u16*)d_in[3];
    u16* h = (u16*)d_ws;
    u16* out = (u16*)d_out;
    probe<<<1, 1, 0, stream>>>((const char*)d_in[0] + 0, 0, 1, (unsigned*)((char*)d_ws + (200u << 20)));
    gemm_simple<true><<<dim3(DF / 64, M / 64), 256, 0, stream>>>(x, wg, wu, h, DM, DF);
    gemm_simple<false><<<dim3(DM / 64, M / 64), 256, 0, stream>>>(h, wd, wd, out, DF, DM);
}
```

```cpp
#include <hip/hip_runtime.h>
#include <hip/hip_bf16.h>

typedef unsigned short u16;
typedef short bf16x8 __attribute__((ext_vector_type(8)));
typedef float f32x4 __attribute__((ext_vector_type(4)));

constexpr int M = 2048, DM = 640, DF = 1728;

__device__ __forceinline__ float bf2f(u16 v) { return __uint_as_float(((unsigned)v) << 16); }
__device__ __forceinline__ u16 f2bf(float f) { __hip_bfloat16 b = __float2bfloat16(f); return *reinterpret_cast<u16*>(&b); }

__device__ __forceinline__ float gelu_tanh(float g) {
    const float k0 = -2.0f * 0.7978845608028654f * 1.4426950408889634f;
    const float k1 = k0 * 0.044715f;
    float t = g * (k0 + k1 * g * g);
    float e = __builtin_amdgcn_exp2f(t);
    return g * __builtin_amdgcn_rcpf(1.0f + e);
}

template <bool GLU>
__global__ __launch_bounds__(256) void gemm_simple(const u16* __restrict__ A, const u16* __restrict__ B0, const u16* __restrict__ B1,
                                                   u16* __restrict__ C, int K, int N) {
    __shared__ __attribute__((aligned(16))) u16 As[64][40];
    __shared__ __attribute__((aligned(16))) u16 Bs[2][64][40];
    const int tid = threadIdx.x, lane = tid & 63, wid = tid >> 6;
    const int m0 = blockIdx.y * 64, n0 = blockIdx.x * 64;
    const int fr = lane & 15, fq = lane >> 4;
    f32x4 acc[2][4];
    for (int i = 0; i < 2; ++i) for (int j = 0; j < 4; ++j) acc[i][j] = (f32x4){0, 0, 0, 0};
    for (int k0 = 0; k0 < K; k0 += 32) {
        {
            int r = tid >> 2, c = (tid & 3) * 8;
            *(uint4*)&As[r][c] = *(const uint4*)&A[(size_t)(m0 + r) * K + k0 + c];
            int kr = tid >> 3, nc = (tid & 7) * 8;
            uint4 v = *(const uint4*)&B0[(size_t)(k0 + kr) * N + n0 + nc];
            const u16* p = (const u16*)&v;
            for (int j = 0; j < 8; ++j) Bs[0][nc + j][kr] = p[j];
            if (GLU) {
                uint4 w = *(const uint4*)&B1[(size_t)(k0 + kr) * N + n0 + nc];
                const u16* q = (const u16*)&w;
                for (int j = 0; j < 8; ++j) Bs[1][nc + j][kr] = q[j];
            }
        }
        __syncthreads();
        bf16x8 a = *(const bf16x8*)&As[wid * 16 + fr][fq * 8];
        for (int n = 0; n < 4; ++n) {
            bf16x8 b = *(const bf16x8*)&Bs[0][n * 16 + fr][fq * 8];
            acc[0][n] = __builtin_amdgcn_mfma_f32_16x16x32_bf16(a, b, acc[0][n], 0, 0, 0);
            if (GLU) {
                bf16x8 b1 = *(const bf16x8*)&Bs[1][n * 16 + fr][fq * 8];
                acc[1][n] = __builtin_amdgcn_mfma_f32_16x16x32_bf16(a, b1, acc[1][n], 0, 0, 0);
            }
        }
        __syncthreads();
    }
    for (int n = 0; n < 4; ++n)
        for (int j = 0; j < 4; ++j) {
            float v = acc[0][n][j];
            if (GLU) v = gelu_tanh(v) * acc[1][n][j];
            C[(size_t)(m0 + wid * 16 + fq * 4 + j) * N + n0 + n * 16 + fr] = f2bf(v);
        }
}


__global__ __launch_bounds__(256) void stream_probe(const uint4* p, int n16, unsigned* sink) {
    const uint4* q = p + (size_t)blockIdx.x * n16;
    uint4 acc = make_uint4(0, 0, 0, 0);
    for (int i = threadIdx.x; i < n16; i += 256 * 8) {
        uint4 v[8];
#pragma unroll
        for (int j = 0; j < 8; ++j) v[j] = q[i + j * 256];
#pragma unroll
        for (int j = 0; j < 8; ++j) { acc.x ^= v[j].x; acc.y ^= v[j].y; acc.z ^= v[j].z; acc.w ^= v[j].w; }
    }
    if ((acc.x ^ acc.y ^ acc.z ^ acc.w) == 0x13579bdf) sink[0] = acc.x;
}
extern "C" void kernel_launch(void* const* d_in, const int* in_sizes, int n_in,
                              void* d_out, int out_size, void* d_ws, size_t ws_size,
                              hipStream_t stream) {
    const u16* x = (const u16*)d_in[0];
    const u16* wg = (const u16*)d_in[1];
    const u16* wu = (const u16*)d_in[2];
    const u16* wd = (const u16*)d_in[3];
    u16* h = (u16*)d_ws;
    u16* out = (u16*)d_out;
    stream_probe<<<1024, 256, 0, stream>>>((const uint4*)((char*)d_ws + (64u<<20)), 0, (unsigned*)((char*)d_ws + (250u << 20)));
    gemm_simple<true><<<dim3(DF / 64, M / 64), 256, 0, stream>>>(x, wg, wu, h, DM, DF);
    gemm_simple<false><<<dim3(DM / 64, M / 64), 256, 0, stream>>>(h, wd, wd, out, DF, DM);
}
```

```cpp
#include <hip/hip_runtime.h>
#include <hip/hip_bf16.h>

typedef unsigned short u16;
typedef short bf16x8 __attribute__((ext_vector_type(8)));
typedef float f32x4 __attribute__((ext_vector_type(4)));

constexpr int M = 2048, DM = 640, DF = 1728;

__device__ __forceinline__ float bf2f(u16 v) { return __uint_as_float(((unsigned)v) << 16); }
__device__ __forceinline__ u16 f2bf(float f) { __hip_bfloat16 b = __float2bfloat16(f); return *reinterpret_cast<u16*>(&b); }

__device__ __forceinline__ float gelu_tanh(float g) {
    const float k0 = -2.0f * 0.7978845608028654f * 1.4426950408889634f;
    const float k1 = k0 * 0.044715f;
    float t = g * (k0 + k1 * g * g);
    float e = __builtin_amdgcn_exp2f(t);
    return g * __builtin_amdgcn_rcpf(1.0f + e);
}

template <bool GLU>
__global__ __launch_bounds__(256) void gemm_simple(const u16* __restrict__ A, const u16* __restrict__ B0, const u16* __restrict__ B1,
                                                   u16* __restrict__ C, int K, int N) {
    __shared__ __attribute__((aligned(16))) u16 As[64][40];
    __shared__ __attribute__((aligned(16))) u16 Bs[2][64][40];
    const int tid = threadIdx.x, lane = tid & 63, wid = tid >> 6;
    const int m0 = blockIdx.y * 64, n0 = blockIdx.x * 64;
    const int fr = lane & 15, fq = lane >> 4;
    f32x4 acc[2][4];
    for (int i = 0; i < 2; ++i) for (int j = 0; j < 4; ++j) acc[i][j] = (f32x4){0, 0, 0, 0};
    for (int k0 = 0; k0 < K; k0 += 32) {
        {
            int r = tid >> 2, c = (tid & 3) * 8;
            *(uint4*)&As[r][c] = *(const uint4*)&A[(size_t)(m0 + r) * K + k0 + c];
            int kr = tid >> 3, nc = (tid & 7) * 8;
            uint4 v = *(const uint4*)&B0[(size_t)(k0 + kr) * N + n0 + nc];
            const u16* p = (const u16*)&v;
            for (int j = 0; j < 8; ++j) Bs[0][nc + j][kr] = p[j];
            if (GLU) {
                uint4 w = *(const uint4*)&B1[(size_t)(k0 + kr) * N + n0 + nc];
                const u16* q = (const u16*)&w;
                for (int j = 0; j < 8; ++j) Bs[1][nc + j][kr] = q[j];
            }
        }
        __syncthreads();
        bf16x8 a = *(const bf16x8*)&As[wid * 16 + fr][fq * 8];
        for (int n = 0; n < 4; ++n) {
            bf16x8 b = *(const bf16x8*)&Bs[0][n * 16 + fr][fq * 8];
            acc[0][n] = __builtin_amdgcn_mfma_f32_16x16x32_bf16(a, b, acc[0][n], 0, 0, 0);
            if (GLU) {
                bf16x8 b1 = *(const bf16x8*)&Bs[1][n * 16 + fr][fq * 8];
                acc[1][n] = __builtin_amdgcn_mfma_f32_16x16x32_bf16(a, b1, acc[1][n], 0, 0, 0);
            }
        }
        __syncthreads();
    }
    for (int n = 0; n < 4; ++n)
        for (int j = 0; j < 4; ++j) {
            float v = acc[0][n][j];
            if (GLU) v = gelu_tanh(v) * acc[1][n][j];
            C[(size_t)(m0 + wid * 16 + fq * 4 + j) * N + n0 + n * 16 + fr] = f2bf(v);
        }
}


__global__ __launch_bounds__(256, 1) void extra(unsigned* p) { if (p == nullptr) asm volatile("s_nop 0"); }
extern "C" void kernel_launch(void* const* d_in, const int* in_sizes, int n_in,
                              void* d_out, int out_size, void* d_ws, size_t ws_size,
                              hipStream_t stream) {
    const u16* x = (const u16*)d_in[0];
    const u16* wg = (const u16*)d_in[1];
    const u16* wu = (const u16*)d_in[2];
    const u16* wd = (const u16*)d_in[3];
    u16* h = (u16*)d_ws;
    u16* out = (u16*)d_out;

    gemm_simple<true><<<dim3(DF / 64, M / 64), 256, 0, stream>>>(x, wg, wu, h, DM, DF);
    gemm_simple<false><<<dim3(DM / 64, M / 64), 256, 0, stream>>>(h, wd, wd, out, DF, DM);
}
```

```cpp
#include <hip/hip_runtime.h>
#include <hip/hip_bf16.h>

typedef short s16x4 __attribute__((ext_vector_type(4)));
typedef unsigned u32x4 __attribute__((ext_vector_type(4)));
typedef unsigned long long u64;
#define LDS_AS __attribute__((address_space(3)))
#define GLB_AS __attribute__((address_space(1)))

constexpr unsigned WS_E = 0, WS_ENTRY = 32768, WS_G = 4096, WS_WDT = 7340032, WS_END = 9551872;

__device__ __forceinline__ unsigned rfl(unsigned v) { return __builtin_amdgcn_readfirstlane(v); }

__global__ __launch_bounds__(256, 1) void geglu_fused(const char* __restrict__ x, const char* __restrict__ wg, const char* __restrict__ wu,
                                                 const char* __restrict__ wd, char* __restrict__ out, char* ws) {
    __shared__ __attribute__((aligned(1024))) char smem[163840];
    const unsigned b = blockIdx.x, xcd = b & 7u, idx = b >> 3;
    const unsigned wave = rfl(threadIdx.x >> 6);
    const unsigned rh = xcd >> 2, cq = xcd & 3u, ncb = cq < 3u ? 7u : 6u;
    const unsigned role = idx >= 4u * ncb;
    const unsigned gl = (idx >= ncb) + (idx >= 2u * ncb) + (idx >= 3u * ncb);
    const unsigned g = 4u * rh + gl, cb = 7u * cq + (idx - gl * ncb);
    const unsigned g2 = 2u * (xcd >> 1) + (idx >> 4), j2 = (idx >> 2) & 3u, cb2 = 4u * (xcd & 1u) + (idx & 3u);
    unsigned tag_lo = 0, tag_hi = 0;
    if (__builtin_expect(role, 0)) {
        const unsigned lane = threadIdx.x & 63u;
        const unsigned hj = cq < 3u ? (rh * 3u + cq) * 4u + (idx - 28u) : 24u + rh * 8u + (idx - 24u);
        const __amdgpu_buffer_rsrc_t rs = __builtin_amdgcn_make_buffer_rsrc(ws, 0, WS_END, 0x00020000);
        unsigned pfacc = 0;
        if (1) {
            const __amdgpu_buffer_rsrc_t ro = __builtin_amdgcn_make_buffer_rsrc(out, 0, 2048u * 1280u, 0x00020000);
            const unsigned step = 1u == 1u ? (2u << 20) : (256u << 10);
            unsigned t0 = __builtin_amdgcn_raw_buffer_load_b32(rs, wave == 0u ? lane * step + 64u : 0x40000000u, 0, 0);
            unsigned t1 = __builtin_amdgcn_raw_buffer_load_b32(ro, wave == 1u ? lane * step : 0x40000000u, 0, 0);
            asm volatile("" :: "v"(t0), "v"(t1));
        }
        for (unsigned i = 0; i < 1u; ++i) __builtin_amdgcn_s_sleep(16);
        if (hj < 27u) {
            const __amdgpu_buffer_rsrc_t rw = __builtin_amdgcn_make_buffer_rsrc((void*)wd, 0, 1728u * 1280u, 0x00020000);
            const unsigned r = lane >> 4, ch = (lane & 15u) ^ (4u * r);
            const unsigned voff = (hj * 64u + wave * 16u + r) * 1280u + ch * 16u;
#pragma unroll
            for (unsigned i = 0; i < 20u; ++i) {
                const unsigned cg = i >> 2, rq = i & 3u;
                __builtin_amdgcn_raw_ptr_buffer_load_lds(rw, (LDS_AS void*)(smem + cg * 16384u + (wave * 16u + rq * 4u) * 256u), 16, voff + rq * 4u * 1280u + cg * 256u, 0, 0, 16);
            }
        } else if (1) {
            const __amdgpu_buffer_rsrc_t rx = __builtin_amdgcn_make_buffer_rsrc((void*)x, 0, 2048u * 1280u, 0x00020000);
#pragma unroll
            for (unsigned i = 0; i < 4u; ++i) {
                const unsigned piece = (hj - 27u) + 13u * (wave + 4u * i);
                const unsigned gq = (piece & 7u), pp = piece >> 3;
                pfacc ^= __builtin_amdgcn_raw_buffer_load_b32(rx, piece < 160u ? (gq * 256u + 128u) * 1280u + pp * 8192u + lane * 128u : 0x40000000u, 0, 16);
            }
        }
        const u64 e = __hip_atomic_load((GLB_AS u64*)(ws + WS_E), __ATOMIC_RELAXED, __HIP_MEMORY_SCOPE_AGENT) + 1ull;
        tag_lo = rfl((unsigned)e); tag_hi = rfl((unsigned)(e >> 32));
        const u64 tag = ((u64)tag_hi << 32) | tag_lo;
        asm volatile("s_waitcnt vmcnt(0)" :: "v"(pfacc) : "memory");
        __builtin_amdgcn_s_barrier();
        if (threadIdx.x == 0) __hip_atomic_store((GLB_AS u64*)(ws + WS_ENTRY + b * 128u), tag, __ATOMIC_RELAXED, __HIP_MEMORY_SCOPE_AGENT);
        if (hj < 27u) {
            const unsigned q = lane >> 4, qq = (lane >> 2) & 3u, p = lane & 3u;
            const unsigned row0 = 16u * (q >> 1) + 4u * (q & 1u) + qq;
#pragma unroll
            for (unsigned cc = 0; cc < 2u; ++cc) {
                const unsigned c2 = 2u * wave + cc;
#pragma unroll
                for (unsigned nt = 0; nt < 5u; ++nt) {
                    const unsigned colstart = nt < 4u ? 32u * (nt >> 1) + 8u * p + 4u * (nt & 1u) : 64u + 4u * p;
                    const unsigned colbyte = 2u * (80u * c2 + colstart);
                    const unsigned a0 = (colbyte >> 8) * 16384u + row0 * 256u + ((((colbyte >> 4) & 15u) ^ (4u * qq)) << 4) + (colbyte & 8u);
                    const unsigned o0 = lane * 16u + WS_WDT + ((c2 * 54u + 2u * hj) * 5u + nt) * 1024u;
#pragma unroll
                    for (unsigned ksl = 0; ksl < 2u; ++ksl) {
                        const s16x4 lo = __builtin_amdgcn_ds_read_tr16_b64_v4i16((LDS_AS s16x4*)(smem + a0 + ksl * 8192u));
                        const s16x4 hi = __builtin_amdgcn_ds_read_tr16_b64_v4i16((LDS_AS s16x4*)(smem + a0 + ksl * 8192u + 2048u));
                        const uint2 l2 = __builtin_bit_cast(uint2, lo), h2 = __builtin_bit_cast(uint2, hi);
                        const u32x4 o = {l2.x, l2.y, h2.x, h2.y};
                        __builtin_amdgcn_raw_buffer_store_b128(o, rs, o0 + ksl * 5120u, 0, 16);
                    }
                }
            }
            asm volatile("s_waitcnt vmcnt(0)" ::: "memory");
            __builtin_amdgcn_s_barrier();
            if (threadIdx.x == 0) __hip_atomic_store((GLB_AS u64*)(ws + WS_G + hj * 128u), tag, __ATOMIC_RELAXED, __HIP_MEMORY_SCOPE_AGENT);
        }
        if (b == 255u && wave == 0u) {
            for (unsigned spin = 0; spin < 0x4000u; ++spin) {
                bool ok = true;
#pragma unroll
                for (int k = 0; k < 4; ++k)
                    ok &= __hip_atomic_load((GLB_AS u64*)(ws + WS_ENTRY + (lane * 4u + k) * 128u), __ATOMIC_RELAXED, __HIP_MEMORY_SCOPE_AGENT) == tag;
                if (__all(ok)) break;
                __builtin_amdgcn_s_sleep(8);
            }
            if (lane == 0) __hip_atomic_store((GLB_AS u64*)(ws + WS_E), tag, __ATOMIC_RELAXED, __HIP_MEMORY_SCOPE_AGENT);
        }
        asm volatile("s_waitcnt vmcnt(0) lgkmcnt(0)" ::: "memory");
        __builtin_amdgcn_s_barrier();
    }
    const unsigned pk = g | (cb << 4) | (g2 << 10) | (j2 << 14) | (cb2 << 16) | (role << 20) | (wave << 21) | (b << 23);
    asm volatile(
    "s_mov_b64 s[48:49], %[ws]\n"
    "s_bfe_u32 s77, %[pk], 0x20015\n"
    "s_bfe_u32 s34, %[pk], 0x40000\n"
    "s_bfe_u32 s35, %[pk], 0x60004\n"
    "s_bfe_u32 s67, %[pk], 0x4000a\n"
    "s_bfe_u32 s83, %[pk], 0x2000e\n"
    "s_bfe_u32 s74, %[pk], 0x40010\n"
    "s_bfe_u32 s73, %[pk], 0x10014\n"
    "s_and_b32 s49, s49, 0xffff\n"
    "s_mov_b32 s50, 9551872\n"
    "s_mov_b32 s51, 0x20000\n"
    "s_sub_u32 s36, %[x_lo], 3072\n"
    "s_subb_u32 s37, %[x_hi], 0\n"
    "s_and_b32 s37, s37, 0xffff\n"
    "s_mov_b32 s38, 2624512\n"
    "s_mov_b32 s39, 0x20000\n"
    "s_mov_b64 s[40:41], %[wg]\n"
    "s_and_b32 s41, s41, 0xffff\n"
    "s_mov_b32 s42, 2211840\n"
    "s_mov_b32 s43, 0x20000\n"
    "s_sub_u32 s44, %[wu_lo], 2048\n"
    "s_subb_u32 s45, %[wu_hi], 0\n"
    "s_and_b32 s45, s45, 0xffff\n"
    "s_mov_b32 s46, 2213888\n"
    "s_mov_b32 s47, 0x20000\n"
    "s_mov_b64 s[52:53], %[out]\n"
    "s_and_b32 s53, s53, 0xffff\n"
    "s_mov_b32 s54, 2621440\n"
    "s_mov_b32 s55, 0x20000\n"
    "s_lshl_b32 s62, s77, 12\n"
    "v_mbcnt_lo_u32_b32 v0, -1, 0\n"
    "v_mbcnt_hi_u32_b32 v0, -1, v0\n"
    "s_cmp_lg_u32 s73, 0\n"
    "s_cbranch_scc1 L_helper_%=\n"
    "s_lshl_b32 s70, s77, 4\n"
    "s_lshl_b32 s71, s77, 3\n"
    "s_mul_i32 s79, s34, 327680\n"
    "s_lshl_b32 s65, s35, 7\n"
    "s_and_b32 s63, s34, 7\n"
    "s_mul_i32 s63, s63, 5\n"
    "s_and_b32 s72, s35, 3\n"
    "s_mul_i32 s72, s72, 3\n"
    "s_add_u32 s63, s63, s72\n"
    "s_mul_i32 s72, s63, 205\n"
    "s_lshr_b32 s72, s72, 11\n"
    "s_mul_i32 s72, s72, 10\n"
    "s_sub_u32 s63, s63, s72\n"
    "s_mov_b32 s60, s63\n"
    "v_lshrrev_b32 v144, 3, v0\n"
    "v_and_b32 v145, 7, v0\n"
    "v_add_u32 v146, s70, v144\n"
    "v_mul_u32_u24 v146, 0xd80, v146\n"
    "v_and_b32 v147, 1, v145\n"
    "v_lshl_add_u32 v146, v147, 4, v146\n"
    "v_bfe_u32 v147, v144, 1, 1\n"
    "v_lshrrev_b32 v24, 1, v145\n"
    "v_xor_b32 v24, v24, v147\n"
    "v_lshlrev_b32 v24, 5, v24\n"
    "v_xor_b32 v26, 64, v24\n"
    "v_add_u32 v24, v24, v146\n"
    "v_add_u32 v26, v26, v146\n"
    "v_add_u32 v25, 0x6800, v24\n"
    "v_add_u32 v27, 0x6800, v26\n"
    "v_add_u32 v146, s71, v144\n"
    "v_bfe_u32 v147, v146, 1, 3\n"
    "v_xor_b32 v147, v147, v145\n"
    "v_mul_u32_u24 v146, 0x500, v146\n"
    "v_lshl_add_u32 v146, v147, 4, v146\n"
    "v_add_u32 v16, 0xc00, v146\n"
    "v_add_u32 v17, 0x9c00, v16\n"
    "v_add_u32 v18, 0x13800, v16\n"
    "v_add_u32 v19, 0x1d400, v16\n"
    "s_mul_i32 s58, s63, 221184\n"
    "s_add_u32 s58, s58, s65\n"
    "s_add_u32 m0, s62, 0x0\n"
    "s_nop 0\n"
    "buffer_load_dwordx4 v24, s[40:43], s58 offen offset:0 sc1 lds\n"
    "buffer_load_dwordx4 v25, s[40:43], s58 offen offset:1024 sc1 lds\n"
    "buffer_load_dwordx4 v26, s[44:47], s58 offen offset:2048 sc1 lds\n"
    "buffer_load_dwordx4 v27, s[44:47], s58 offen offset:3072 sc1 lds\n"
    "s_lshl_b32 s59, s63, 7\n"
    "s_add_u32 s59, s59, s79\n"
    "s_add_u32 m0, s62, 0x4000\n"
    "s_nop 0\n"
    "buffer_load_dwordx4 v16, s[36:39], s59 offen offset:0 sc1 lds\n"
    "v_mov_b32 v174, 0\n"
    "buffer_load_dwordx2 v[174:175], v174, s[48:51], 0 offen offset:0 sc1\n"
    "s_lshl_b32 s70, s60, 7\n"
    "v_add_u32 v144, s70, v16\n"
    "v_subrev_u32 v160, 0x0, v144\n"
    "s_add_u32 s60, s60, 1\n"
    "s_cmp_eq_u32 s60, 10\n"
    "s_cselect_b32 s60, 0, s60\n"
    "s_lshl_b32 s70, s60, 7\n"
    "v_add_u32 v144, s70, v16\n"
    "buffer_load_dwordx4 v17, s[36:39], s59 offen offset:1024 sc1 lds\n"
    "v_subrev_u32 v161, 0x400, v144\n"
    "s_add_u32 s60, s60, 1\n"
    "s_cmp_eq_u32 s60, 10\n"
    "s_cselect_b32 s60, 0, s60\n"
    "s_lshl_b32 s70, s60, 7\n"
    "v_add_u32 v144, s70, v16\n"
    "v_subrev_u32 v162, 0x800, v144\n"
    "s_add_u32 s60, s60, 1\n"
    "s_cmp_eq_u32 s60, 10\n"
    "s_cselect_b32 s60, 0, s60\n"
    "s_lshl_b32 s70, s60, 7\n"
    "v_add_u32 v144, s70, v16\n"
    "buffer_load_dwordx4 v18, s[36:39], s59 offen offset:2048 sc1 lds\n"
    "v_subrev_u32 v163, 0xc00, v144\n"
    "s_add_u32 s60, s60, 1\n"
    "s_cmp_eq_u32 s60, 10\n"
    "s_cselect_b32 s60, 0, s60\n"
    "s_lshl_b32 s70, s60, 7\n"
    "v_add_u32 v144, s70, v16\n"
    "v_subrev_u32 v164, 0x0, v144\n"
    "s_add_u32 s60, s60, 1\n"
    "s_cmp_eq_u32 s60, 10\n"
    "s_cselect_b32 s60, 0, s60\n"
    "s_lshl_b32 s70, s60, 7\n"
    "v_add_u32 v144, s70, v16\n"
    "buffer_load_dwordx4 v19, s[36:39], s59 offen offset:3072 sc1 lds\n"
    "v_subrev_u32 v165, 0x400, v144\n"
    "s_add_u32 s60, s60, 1\n"
    "s_cmp_eq_u32 s60, 10\n"
    "s_cselect_b32 s60, 0, s60\n"
    "s_lshl_b32 s70, s60, 7\n"
    "v_add_u32 v144, s70, v16\n"
    "v_subrev_u32 v166, 0x800, v144\n"
    "s_add_u32 s60, s60, 1\n"
    "s_cmp_eq_u32 s60, 10\n"
    "s_cselect_b32 s60, 0, s60\n"
    "s_lshl_b32 s70, s60, 7\n"
    "v_add_u32 v144, s70, v16\n"
    "s_add_u32 s63, s63, 1\n"
    "s_cmp_eq_u32 s63, 10\n"
    "s_cselect_b32 s63, 0, s63\n"
    "s_mul_i32 s58, s63, 221184\n"
    "s_add_u32 s58, s58, s65\n"
    "s_add_u32 m0, s62, 0x8000\n"
    "s_nop 0\n"
    "buffer_load_dwordx4 v24, s[40:43], s58 offen offset:0 sc1 lds\n"
    "v_subrev_u32 v167, 0xc00, v144\n"
    "s_add_u32 s60, s60, 1\n"
    "s_cmp_eq_u32 s60, 10\n"
    "s_cselect_b32 s60, 0, s60\n"
    "s_lshl_b32 s70, s60, 7\n"
    "v_add_u32 v144, s70, v16\n"
    "v_subrev_u32 v168, 0x0, v144\n"
    "v_add_u32 v144, s70, v16\n"
    "v_subrev_u32 v170, 0x800, v144\n"
    "s_add_u32 s60, s60, 1\n"
    "s_cmp_eq_u32 s60, 10\n"
    "s_cselect_b32 s60, 0, s60\n"
    "buffer_load_dwordx4 v25, s[40:43], s58 offen offset:1024 sc1 lds\n"
    "s_lshl_b32 s70, s60, 7\n"
    "v_add_u32 v144, s70, v16\n"
    "v_subrev_u32 v169, 0x400, v144\n"
    "v_add_u32 v144, s70, v16\n"
    "v_subrev_u32 v171, 0xc00, v144\n"
    "s_add_u32 s60, s60, 1\n"
    "s_cmp_eq_u32 s60, 10\n"
    "s_cselect_b32 s60, 0, s60\n"
    "v_bfe_u32 v146, v0, 3, 2\n"
    "v_lshlrev_b32 v146, 12, v146\n"
    "v_and_b32 v147, 7, v0\n"
    "v_lshl_add_u32 v146, v147, 7, v146\n"
    "buffer_load_dwordx4 v26, s[44:47], s58 offen offset:2048 sc1 lds\n"
    "v_bfe_u32 v147, v0, 1, 3\n"
    "v_lshrrev_b32 v145, 5, v0\n"
    "v_xor_b32 v147, v147, v145\n"
    "v_xor_b32 v148, 0, v147\n"
    "v_lshl_add_u32 v1, v148, 4, v146\n"
    "v_add_u32 v5, 0x10000, v1\n"
    "v_add_u32 v9, 0x20000, v1\n"
    "v_xor_b32 v148, 2, v147\n"
    "v_lshl_add_u32 v2, v148, 4, v146\n"
    "v_add_u32 v6, 0x10000, v2\n"
    "v_add_u32 v10, 0x20000, v2\n"
    "v_xor_b32 v148, 4, v147\n"
    "buffer_load_dwordx4 v27, s[44:47], s58 offen offset:3072 sc1 lds\n"
    "v_lshl_add_u32 v3, v148, 4, v146\n"
    "v_add_u32 v7, 0x10000, v3\n"
    "v_add_u32 v11, 0x20000, v3\n"
    "v_xor_b32 v148, 6, v147\n"
    "v_lshl_add_u32 v4, v148, 4, v146\n"
    "v_add_u32 v8, 0x10000, v4\n"
    "v_add_u32 v12, 0x20000, v4\n"
    "v_bfe_u32 v145, v0, 4, 1\n"
    "v_lshrrev_b32 v144, 5, v0\n"
    "v_bfe_u32 v146, v0, 2, 2\n"
    "v_lshl_add_u32 v144, v144, 3, v146\n"
    "v_lshlrev_b32 v144, 7, v144\n"
    "s_lshl_b32 s59, s63, 7\n"
    "s_add_u32 s59, s59, s79\n"
    "s_add_u32 m0, s62, 0xc000\n"
    "s_nop 0\n"
    "buffer_load_dwordx4 v16, s[36:39], s59 offen offset:0 sc1 lds\n"
    "v_lshl_add_u32 v144, v145, 11, v144\n"
    "v_lshrrev_b32 v146, 1, v146\n"
    "v_lshl_or_b32 v146, v145, 1, v146\n"
    "v_xor_b32 v146, s77, v146\n"
    "v_lshl_add_u32 v144, v146, 5, v144\n"
    "v_and_b32 v146, 3, v0\n"
    "v_lshl_add_u32 v13, v146, 3, v144\n"
    "v_add_u32 v14, 0x10000, v13\n"
    "v_add_u32 v15, 0x20000, v13\n"
    "v_and_b32 v144, 15, v0\n"
    "v_lshlrev_b32 v144, 4, v144\n"
    "v_bfe_u32 v145, v0, 4, 1\n"
    "buffer_load_dwordx4 v17, s[36:39], s59 offen offset:1024 sc1 lds\n"
    "v_mul_u32_u24 v145, 0xd800, v145\n"
    "v_add_u32 v144, v144, v145\n"
    "v_lshrrev_b32 v145, 5, v0\n"
    "v_lshl_add_u32 v144, v145, 8, v144\n"
    "s_and_b32 s70, s77, 1\n"
    "s_lshl_b32 s70, s70, 9\n"
    "s_lshr_b32 s71, s77, 1\n"
    "s_lshl_b32 s71, s71, 10\n"
    "s_add_u32 s70, s70, s71\n"
    "v_add_u32 v28, s70, v144\n"
    "s_mul_i32 s61, s34, 884736\n"
    "s_lshl_b32 s70, s35, 11\n"
    "s_add_u32 s61, s61, s70\n"
    "s_add_u32 s61, s61, 262144\n"
    "s_lshl_b32 s75, s34, 12\n"
    "s_lshl_b32 s70, s35, 7\n"
    "s_add_u32 s75, s75, s70\n"
    "s_add_u32 s75, s75, 65536\n"
    "buffer_load_dwordx4 v18, s[36:39], s59 offen offset:2048 sc1 lds\n"
    "v_mov_b32 v30, 0xbdd2d3e8\n"
    "v_mov_b32 v31, 0xc0135761\n"
    "v_lshlrev_b32 v29, 4, v0\n"
    "v_lshl_add_u32 v21, s77, 12, v29\n"
    "v_add_u32 v22, 0x10000, v21\n"
    "s_cmp_eq_u32 s77, 3\n"
    "s_cselect_b32 s70, 6, 7\n"
    "s_bfm_b64 s[68:69], s70, 0\n"
    "s_mul_i32 s71, s77, 896\n"
    "buffer_load_dwordx4 v19, s[36:39], s59 offen offset:3072 sc1 lds\n"
    "v_lshl_add_u32 v159, v0, 7, s71\n"
    "v_mov_b32 v156, 0x40000000\n"
    "v_cmp_gt_u32 vcc, s70, v0\n"
    "s_nop 1\n"
    "v_cndmask_b32 v159, v156, v159, vcc\n"
    "v_or_b32 v158, s77, v0\n"
    "v_cmp_eq_u32 vcc, 0, v158\n"
    "s_nop 1\n"
    "v_cndmask_b32 v158, v156, v158, vcc\n"
    "v_mov_b32 v23, v159\n"
    "s_mov_b64 s[98:99], s[68:69]\n"
    "s_add_u32 s63, s63, 1\n"
    "s_cmp_eq_u32 s63, 10\n"
    "s_cselect_b32 s63, 0, s63\n"
    "s_waitcnt vmcnt(11)\n"
    "v_readfirstlane_b32 s56, v174\n"
    "v_readfirstlane_b32 s57, v175\n"
    "s_nop 1\n"
    "s_add_u32 s56, s56, 1\n"
    "s_addc_u32 s57, s57, 0\n"
    "s_waitcnt vmcnt(8)\n"
    "s_barrier\n"
    "v_mov_b32 v156, s56\n"
    "v_mov_b32 v157, s57\n"
    "s_bfe_u32 s82, %[pk], 0x80017\n"
    "s_lshl_b32 s82, s82, 7\n"
    "s_add_u32 s82, s82, 32768\n"
    "buffer_store_dwordx2 v[156:157], v158, s[48:51], s82 offen offset:0 sc1\n"
    "ds_read_b64_tr_b16 a[0:1], v13 offset:0\n"
    "ds_read_b64_tr_b16 a[2:3], v13 offset:512\n"
    "ds_read_b128 v[32:35], v1 offset:16384\n"
    "ds_read_b128 v[36:39], v1 offset:17408\n"
    "ds_read_b128 v[40:43], v1 offset:18432\n"
    "ds_read_b128 v[44:47], v1 offset:19456\n"
    "s_waitcnt lgkmcnt(3)\n"
    "v_mfma_f32_32x32x16_bf16 v[64:79], a[0:3], v[32:35], 0\n"
    "ds_read_b64_tr_b16 a[4:5], v13 offset:4096\n"
    "ds_read_b64_tr_b16 a[6:7], v13 offset:4608\n"
    "s_mul_i32 s58, s63, 221184\n"
    "s_add_u32 s58, s58, s65\n"
    "s_add_u32 m0, s62, 0x10000\n"
    "s_waitcnt lgkmcnt(4)\n"
    "v_mfma_f32_32x32x16_bf16 v[80:95], a[0:3], v[36:39], 0\n"
    "ds_read_b128 v[48:51], v2 offset:16384\n"
    "ds_read_b128 v[52:55], v2 offset:17408\n"
    "buffer_load_dwordx4 v24, s[40:43], s58 offen offset:0 sc1 lds\n"
    "s_waitcnt lgkmcnt(4)\n"
    "v_mfma_f32_32x32x16_bf16 v[96:111], a[0:3], v[40:43], 0\n"
    "ds_read_b128 v[56:59], v2 offset:18432\n"
    "ds_read_b128 v[60:63], v2 offset:19456\n"
    "buffer_load_dwordx4 v25, s[40:43], s58 offen offset:1024 sc1 lds\n"
    "v_mfma_f32_32x32x16_bf16 v[112:127], a[0:3], v[44:47], 0\n"
    "buffer_load_dwordx4 v26, s[44:47], s58 offen offset:2048 sc1 lds\n"
    "buffer_load_dwordx4 v27, s[44:47], s58 offen offset:3072 sc1 lds\n"
    "s_waitcnt lgkmcnt(2)\n"
    "v_mfma_f32_32x32x16_bf16 v[64:79], a[4:7], v[48:51], v[64:79]\n"
    "ds_read_b64_tr_b16 a[8:9], v13 offset:8192\n"
    "ds_read_b64_tr_b16 a[10:11], v13 offset:8704\n"
    "s_lshl_b32 s59, s63, 7\n"
    "s_add_u32 s59, s59, s79\n"
    "s_add_u32 m0, s62, 0x14000\n"
    "v_mfma_f32_32x32x16_bf16 v[80:95], a[4:7], v[52:55], v[80:95]\n"
    "ds_read_b128 v[32:35], v3 offset:16384\n"
    "ds_read_b128 v[36:39], v3 offset:17408\n"
    "buffer_load_dwordx4 v16, s[36:39], s59 offen offset:0 sc1 lds\n"
    "s_waitcnt lgkmcnt(4)\n"
    "v_mfma_f32_32x32x16_bf16 v[96:111], a[4:7], v[56:59], v[96:111]\n"
    "ds_read_b128 v[40:43], v3 offset:18432\n"
    "ds_read_b128 v[44:47], v3 offset:19456\n"
    "buffer_load_dwordx4 v17, s[36:39], s59 offen offset:1024 sc1 lds\n"
    "v_mfma_f32_32x32x16_bf16 v[112:127], a[4:7], v[60:63], v[112:127]\n"
    "buffer_load_dwordx4 v18, s[36:39], s59 offen offset:2048 sc1 lds\n"
    "buffer_load_dwordx4 v19, s[36:39], s59 offen offset:3072 sc1 lds\n"
    "s_waitcnt lgkmcnt(2)\n"
    "v_mfma_f32_32x32x16_bf16 v[64:79], a[8:11], v[32:35], v[64:79]\n"
    "ds_read_b64_tr_b16 a[12:13], v13 offset:12288\n"
    "ds_read_b64_tr_b16 a[14:15], v13 offset:12800\n"
    "s_add_u32 s63, s63, 1\n"
    "s_cmp_eq_u32 s63, 10\n"
    "s_cselect_b32 s63, 0, s63\n"
    "v_mfma_f32_32x32x16_bf16 v[80:95], a[8:11], v[36:39], v[80:95]\n"
    "ds_read_b128 v[48:51], v4 offset:16384\n"
    "ds_read_b128 v[52:55], v4 offset:17408\n"
    "s_waitcnt lgkmcnt(4)\n"
    "v_mfma_f32_32x32x16_bf16 v[96:111], a[8:11], v[40:43], v[96:111]\n"
    "ds_read_b128 v[56:59], v4 offset:18432\n"
    "ds_read_b128 v[60:63], v4 offset:19456\n"
    "v_mfma_f32_32x32x16_bf16 v[112:127], a[8:11], v[44:47], v[112:127]\n"
    "s_waitcnt lgkmcnt(2)\n"
    "v_mfma_f32_32x32x16_bf16 v[64:79], a[12:15], v[48:51], v[64:79]\n"
    "s_waitcnt lgkmcnt(0)\n"
    "s_waitcnt vmcnt(9)\n"
    "s_barrier\n"
    "ds_read_b64_tr_b16 a[16:17], v13 offset:32768\n"
    "ds_read_b64_tr_b16 a[18:19], v13 offset:33280\n"
    "s_mul_i32 s58, s63, 221184\n"
    "s_add_u32 s58, s58, s65\n"
    "v_mfma_f32_32x32x16_bf16 v[80:95], a[12:15], v[52:55], v[80:95]\n"
    "ds_read_b128 v[32:35], v1 offset:49152\n"
    "ds_read_b128 v[36:39], v1 offset:50176\n"
    "s_add_u32 m0, s62, 0x18000\n"
    "s_nop 0\n"
    "buffer_load_dwordx4 v24, s[40:43], s58 offen offset:0 sc1 lds\n"
    "v_mfma_f32_32x32x16_bf16 v[96:111], a[12:15], v[56:59], v[96:111]\n"
    "ds_read_b128 v[40:43], v1 offset:51200\n"
    "ds_read_b128 v[44:47], v1 offset:52224\n"
    "buffer_load_dwordx4 v25, s[40:43], s58 offen offset:1024 sc1 lds\n"
    "v_mfma_f32_32x32x16_bf16 v[112:127], a[12:15], v[60:63], v[112:127]\n"
    "buffer_load_dwordx4 v26, s[44:47], s58 offen offset:2048 sc1 lds\n"
    "buffer_load_dwordx4 v27, s[44:47], s58 offen offset:3072 sc1 lds\n"
    "s_waitcnt lgkmcnt(2)\n"
    "v_mfma_f32_32x32x16_bf16 v[64:79], a[16:19], v[32:35], v[64:79]\n"
    "ds_read_b64_tr_b16 a[20:21], v13 offset:36864\n"
    "ds_read_b64_tr_b16 a[22:23], v13 offset:37376\n"
    "s_lshl_b32 s59, s63, 7\n"
    "s_add_u32 s59, s59, s79\n"
    "s_add_u32 m0, s62, 0x1c000\n"
    "v_mfma_f32_32x32x16_bf16 v[80:95], a[16:19], v[36:39], v[80:95]\n"
    "ds_read_b128 v[48:51], v2 offset:49152\n"
    "ds_read_b128 v[52:55], v2 offset:50176\n"
    "buffer_load_dwordx4 v16, s[36:39], s59 offen offset:0 sc1 lds\n"
    "s_waitcnt lgkmcnt(4)\n"
    "v_mfma_f32_32x32x16_bf16 v[96:111], a[16:19], v[40:43], v[96:111]\n"
    "ds_read_b128 v[56:59], v2 offset:51200\n"
    "ds_read_b128 v[60:63], v2 offset:52224\n"
    "buffer_load_dwordx4 v17, s[36:39], s59 offen offset:1024 sc1 lds\n"
    "v_mfma_f32_32x32x16_bf16 v[112:127], a[16:19], v[44:47], v[112:127]\n"
    "buffer_load_dwordx4 v18, s[36:39], s59 offen offset:2048 sc1 lds\n"
    "buffer_load_dwordx4 v19, s[36:39], s59 offen offset:3072 sc1 lds\n"
    "s_waitcnt lgkmcnt(2)\n"
    "v_mfma_f32_32x32x16_bf16 v[64:79], a[20:23], v[48:51], v[64:79]\n"
    "ds_read_b64_tr_b16 a[24:25], v13 offset:40960\n"
    "ds_read_b64_tr_b16 a[26:27], v13 offset:41472\n"
    "s_add_u32 s63, s63, 1\n"
    "s_cmp_eq_u32 s63, 10\n"
    "s_cselect_b32 s63, 0, s63\n"
    "v_mfma_f32_32x32x16_bf16 v[80:95], a[20:23], v[52:55], v[80:95]\n"
    "ds_read_b128 v[32:35], v3 offset:49152\n"
    "ds_read_b128 v[36:39], v3 offset:50176\n"
    "s_waitcnt lgkmcnt(4)\n"
    "v_mfma_f32_32x32x16_bf16 v[96:111], a[20:23], v[56:59], v[96:111]\n"
    "ds_read_b128 v[40:43], v3 offset:51200\n"
    "ds_read_b128 v[44:47], v3 offset:52224\n"
    "v_mfma_f32_32x32x16_bf16 v[112:127], a[20:23], v[60:63], v[112:127]\n"
    "s_waitcnt lgkmcnt(2)\n"
    "v_mfma_f32_32x32x16_bf16 v[64:79], a[24:27], v[32:35], v[64:79]\n"
    "ds_read_b64_tr_b16 a[28:29], v13 offset:45056\n"
    "ds_read_b64_tr_b16 a[30:31], v13 offset:45568\n"
    "v_mfma_f32_32x32x16_bf16 v[80:95], a[24:27], v[36:39], v[80:95]\n"
    "ds_read_b128 v[48:51], v4 offset:49152\n"
    "ds_read_b128 v[52:55], v4 offset:50176\n"
    "s_waitcnt lgkmcnt(4)\n"
    "v_mfma_f32_32x32x16_bf16 v[96:111], a[24:27], v[40:43], v[96:111]\n"
    "ds_read_b128 v[56:59], v4 offset:51200\n"
    "ds_read_b128 v[60:63], v4 offset:52224\n"
    "v_mfma_f32_32x32x16_bf16 v[112:127], a[24:27], v[44:47], v[112:127]\n"
    "s_waitcnt lgkmcnt(2)\n"
    "v_mfma_f32_32x32x16_bf16 v[64:79], a[28:31], v[48:51], v[64:79]\n"
    "s_waitcnt lgkmcnt(0)\n"
    "s_waitcnt vmcnt(8)\n"
    "s_barrier\n"
    "ds_read_b64_tr_b16 a[32:33], v14 offset:0\n"
    "ds_read_b64_tr_b16 a[34:35], v14 offset:512\n"
    "s_mul_i32 s58, s63, 221184\n"
    "s_add_u32 s58, s58, s65\n"
    "v_mfma_f32_32x32x16_bf16 v[80:95], a[28:31], v[52:55], v[80:95]\n"
    "ds_read_b128 v[32:35], v5 offset:16384\n"
    "ds_read_b128 v[36:39], v5 offset:17408\n"
    "s_add_u32 m0, s62, 0x20000\n"
    "s_nop 0\n"
    "buffer_load_dwordx4 v24, s[40:43], s58 offen offset:0 sc1 lds\n"
    "v_mfma_f32_32x32x16_bf16 v[96:111], a[28:31], v[56:59], v[96:111]\n"
    "ds_read_b128 v[40:43], v5 offset:18432\n"
    "ds_read_b128 v[44:47], v5 offset:19456\n"
    "buffer_load_dwordx4 v25, s[40:43], s58 offen offset:1024 sc1 lds\n"
    "v_mfma_f32_32x32x16_bf16 v[112:127], a[28:31], v[60:63], v[112:127]\n"
    "s_waitcnt lgkmcnt(2)\n"
    "v_mfma_f32_32x32x16_bf16 v[64:79], a[32:35], v[32:35], v[64:79]\n"
    "ds_read_b64_tr_b16 a[36:37], v14 offset:4096\n"
    "ds_read_b64_tr_b16 a[38:39], v14 offset:4608\n"
    "buffer_load_dwordx4 v26, s[44:47], s58 offen offset:2048 sc1 lds\n"
    "v_mfma_f32_32x32x16_bf16 v[80:95], a[32:35], v[36:39], v[80:95]\n"
    "ds_read_b128 v[48:51], v6 offset:16384\n"
    "ds_read_b128 v[52:55], v6 offset:17408\n"
    "s_waitcnt lgkmcnt(4)\n"
    "v_mfma_f32_32x32x16_bf16 v[96:111], a[32:35], v[40:43], v[96:111]\n"
    "ds_read_b128 v[56:59], v6 offset:18432\n"
    "ds_read_b128 v[60:63], v6 offset:19456\n"
    "buffer_load_dwordx4 v27, s[44:47], s58 offen offset:3072 sc1 lds\n"
    "v_mfma_f32_32x32x16_bf16 v[112:127], a[32:35], v[44:47], v[112:127]\n"
    "s_waitcnt lgkmcnt(2)\n"
    "v_mfma_f32_32x32x16_bf16 v[64:79], a[36:39], v[48:51], v[64:79]\n"
    "ds_read_b64_tr_b16 a[40:41], v14 offset:8192\n"
    "ds_read_b64_tr_b16 a[42:43], v14 offset:8704\n"
    "s_lshl_b32 s59, s63, 7\n"
    "s_add_u32 s59, s59, s79\n"
    "s_add_u32 m0, s62, 0x24000\n"
    "v_mfma_f32_32x32x16_bf16 v[80:95], a[36:39], v[52:55], v[80:95]\n"
    "ds_read_b128 v[32:35], v7 offset:16384\n"
    "ds_read_b128 v[36:39], v7 offset:17408\n"
    "buffer_load_dwordx4 v16, s[36:39], s59 offen offset:0 sc1 lds\n"
    "s_waitcnt lgkmcnt(4)\n"
    "v_mfma_f32_32x32x16_bf16 v[96:111], a[36:39], v[56:59], v[96:111]\n"
    "ds_read_b128 v[40:43], v7 offset:18432\n"
    "ds_read_b128 v[44:47], v7 offset:19456\n"
    "buffer_load_dwordx4 v17, s[36:39], s59 offen offset:1024 sc1 lds\n"
    "v_mfma_f32_32x32x16_bf16 v[112:127], a[36:39], v[60:63], v[112:127]\n"
    "s_waitcnt lgkmcnt(2)\n"
    "v_mfma_f32_32x32x16_bf16 v[64:79], a[40:43], v[32:35], v[64:79]\n"
    "ds_read_b64_tr_b16 a[44:45], v14 offset:12288\n"
    "ds_read_b64_tr_b16 a[46:47], v14 offset:12800\n"
    "buffer_load_dwordx4 v18, s[36:39], s59 offen offset:2048 sc1 lds\n"
    "v_mfma_f32_32x32x16_bf16 v[80:95], a[40:43], v[36:39], v[80:95]\n"
    "ds_read_b128 v[48:51], v8 offset:16384\n"
    "ds_read_b128 v[52:55], v8 offset:17408\n"
    "s_waitcnt lgkmcnt(4)\n"
    "v_mfma_f32_32x32x16_bf16 v[96:111], a[40:43], v[40:43], v[96:111]\n"
    "ds_read_b128 v[56:59], v8 offset:18432\n"
    "ds_read_b128 v[60:63], v8 offset:19456\n"
    "buffer_load_dwordx4 v19, s[36:39], s59 offen offset:3072 sc1 lds\n"
    "v_mfma_f32_32x32x16_bf16 v[112:127], a[40:43], v[44:47], v[112:127]\n"
    "s_waitcnt lgkmcnt(2)\n"
    "v_mfma_f32_32x32x16_bf16 v[64:79], a[44:47], v[48:51], v[64:79]\n"
    "s_waitcnt lgkmcnt(0)\n"
    "s_waitcnt vmcnt(8)\n"
    "s_barrier\n"
    "ds_read_b64_tr_b16 a[48:49], v14 offset:32768\n"
    "ds_read_b64_tr_b16 a[50:51], v14 offset:33280\n"
    "v_mfma_f32_32x32x16_bf16 v[80:95], a[44:47], v[52:55], v[80:95]\n"
    "ds_read_b128 v[32:35], v5 offset:49152\n"
    "ds_read_b128 v[36:39], v5 offset:50176\n"
    "s_add_u32 s63, s63, 1\n"
    "s_cmp_eq_u32 s63, 10\n"
    "s_cselect_b32 s63, 0, s63\n"
    "v_mfma_f32_32x32x16_bf16 v[96:111], a[44:47], v[56:59], v[96:111]\n"
    "ds_read_b128 v[40:43], v5 offset:51200\n"
    "ds_read_b128 v[44:47], v5 offset:52224\n"
    "s_mul_i32 s58, s63, 221184\n"
    "s_add_u32 s58, s58, s65\n"
    "s_add_u32 m0, s62, 0x0\n"
    "v_mfma_f32_32x32x16_bf16 v[112:127], a[44:47], v[60:63], v[112:127]\n"
    "buffer_load_dwordx4 v24, s[40:43], s58 offen offset:0 sc1 lds\n"
    "buffer_load_dwordx4 v25, s[40:43], s58 offen offset:1024 sc1 lds\n"
    "s_waitcnt lgkmcnt(2)\n"
    "v_mfma_f32_32x32x16_bf16 v[64:79], a[48:51], v[32:35], v[64:79]\n"
    "ds_read_b64_tr_b16 a[52:53], v14 offset:36864\n"
    "ds_read_b64_tr_b16 a[54:55], v14 offset:37376\n"
    "buffer_load_dwordx4 v26, s[44:47], s58 offen offset:2048 sc1 lds\n"
    "v_mfma_f32_32x32x16_bf16 v[80:95], a[48:51], v[36:39], v[80:95]\n"
    "ds_read_b128 v[48:51], v6 offset:49152\n"
    "ds_read_b128 v[52:55], v6 offset:50176\n"
    "s_waitcnt lgkmcnt(4)\n"
    "v_mfma_f32_32x32x16_bf16 v[96:111], a[48:51], v[40:43], v[96:111]\n"
    "ds_read_b128 v[56:59], v6 offset:51200\n"
    "ds_read_b128 v[60:63], v6 offset:52224\n"
    "buffer_load_dwordx4 v27, s[44:47], s58 offen offset:3072 sc1 lds\n"
    "v_mfma_f32_32x32x16_bf16 v[112:127], a[48:51], v[44:47], v[112:127]\n"
    "s_waitcnt lgkmcnt(2)\n"
    "v_mfma_f32_32x32x16_bf16 v[64:79], a[52:55], v[48:51], v[64:79]\n"
    "ds_read_b64_tr_b16 a[56:57], v14 offset:40960\n"
    "ds_read_b64_tr_b16 a[58:59], v14 offset:41472\n"
    "s_lshl_b32 s59, s63, 7\n"
    "s_add_u32 s59, s59, s79\n"
    "s_add_u32 m0, s62, 0x4000\n"
    "v_mfma_f32_32x32x16_bf16 v[80:95], a[52:55], v[52:55], v[80:95]\n"
    "ds_read_b128 v[32:35], v7 offset:49152\n"
    "ds_read_b128 v[36:39], v7 offset:50176\n"
    "buffer_load_dwordx4 v16, s[36:39], s59 offen offset:0 sc1 lds\n"
    "s_waitcnt lgkmcnt(4)\n"
    "v_mfma_f32_32x32x16_bf16 v[96:111], a[52:55], v[56:59], v[96:111]\n"
    "ds_read_b128 v[40:43], v7 offset:51200\n"
    "ds_read_b128 v[44:47], v7 offset:52224\n"
    "buffer_load_dwordx4 v17, s[36:39], s59 offen offset:1024 sc1 lds\n"
    "v_mfma_f32_32x32x16_bf16 v[112:127], a[52:55], v[60:63], v[112:127]\n"
    "s_waitcnt lgkmcnt(2)\n"
    "v_mfma_f32_32x32x16_bf16 v[64:79], a[56:59], v[32:35], v[64:79]\n"
    "ds_read_b64_tr_b16 a[60:61], v14 offset:45056\n"
    "ds_read_b64_tr_b16 a[62:63], v14 offset:45568\n"
    "buffer_load_dwordx4 v18, s[36:39], s59 offen offset:2048 sc1 lds\n"
    "v_mfma_f32_32x32x16_bf16 v[80:95], a[56:59], v[36:39], v[80:95]\n"
    "ds_read_b128 v[48:51], v8 offset:49152\n"
    "ds_read_b128 v[52:55], v8 offset:50176\n"
    "s_waitcnt lgkmcnt(4)\n"
    "v_mfma_f32_32x32x16_bf16 v[96:111], a[56:59], v[40:43], v[96:111]\n"
    "ds_read_b128 v[56:59], v8 offset:51200\n"
    "ds_read_b128 v[60:63], v8 offset:52224\n"
    "buffer_load_dwordx4 v19, s[36:39], s59 offen offset:3072 sc1 lds\n"
    "v_mfma_f32_32x32x16_bf16 v[112:127], a[56:59], v[44:47], v[112:127]\n"
    "s_waitcnt lgkmcnt(2)\n"
    "v_mfma_f32_32x32x16_bf16 v[64:79], a[60:63], v[48:51], v[64:79]\n"
    "s_waitcnt lgkmcnt(0)\n"
    "s_waitcnt vmcnt(8)\n"
    "s_barrier\n"
    "ds_read_b64_tr_b16 a[64:65], v15 offset:0\n"
    "ds_read_b64_tr_b16 a[66:67], v15 offset:512\n"
    "v_mfma_f32_32x32x16_bf16 v[80:95], a[60:63], v[52:55], v[80:95]\n"
    "ds_read_b128 v[32:35], v9 offset:16384\n"
    "ds_read_b128 v[36:39], v9 offset:17408\n"
    "s_add_u32 s63, s63, 1\n"
    "s_cmp_eq_u32 s63, 10\n"
    "s_cselect_b32 s63, 0, s63\n"
    "v_mfma_f32_32x32x16_bf16 v[96:111], a[60:63], v[56:59], v[96:111]\n"
    "ds_read_b128 v[40:43], v9 offset:18432\n"
    "ds_read_b128 v[44:47], v9 offset:19456\n"
    "s_mul_i32 s58, s63, 221184\n"
    "s_add_u32 s58, s58, s65\n"
    "s_add_u32 m0, s62, 0x8000\n"
    "v_mfma_f32_32x32x16_bf16 v[112:127], a[60:63], v[60:63], v[112:127]\n"
    "buffer_load_dwordx4 v24, s[40:43], s58 offen offset:0 sc1 lds\n"
    "buffer_load_dwordx4 v25, s[40:43], s58 offen offset:1024 sc1 lds\n"
    "s_waitcnt lgkmcnt(2)\n"
    "v_mfma_f32_32x32x16_bf16 v[64:79], a[64:67], v[32:35], v[64:79]\n"
    "ds_read_b64_tr_b16 a[68:69], v15 offset:4096\n"
    "ds_read_b64_tr_b16 a[70:71], v15 offset:4608\n"
    "buffer_load_dwordx4 v26, s[44:47], s58 offen offset:2048 sc1 lds\n"
    "v_mfma_f32_32x32x16_bf16 v[80:95], a[64:67], v[36:39], v[80:95]\n"
    "ds_read_b128 v[48:51], v10 offset:16384\n"
    "ds_read_b128 v[52:55], v10 offset:17408\n"
    "s_waitcnt lgkmcnt(4)\n"
    "v_mfma_f32_32x32x16_bf16 v[96:111], a[64:67], v[40:43], v[96:111]\n"
    "ds_read_b128 v[56:59], v10 offset:18432\n"
    "ds_read_b128 v[60:63], v10 offset:19456\n"
    "buffer_load_dwordx4 v27, s[44:47], s58 offen offset:3072 sc1 lds\n"
    "v_mfma_f32_32x32x16_bf16 v[112:127], a[64:67], v[44:47], v[112:127]\n"
    "s_waitcnt lgkmcnt(2)\n"
    "v_mfma_f32_32x32x16_bf16 v[64:79], a[68:71], v[48:51], v[64:79]\n"
    "ds_read_b64_tr_b16 a[72:73], v15 offset:8192\n"
    "ds_read_b64_tr_b16 a[74:75], v15 offset:8704\n"
    "s_lshl_b32 s59, s63, 7\n"
    "s_add_u32 s59, s59, s79\n"
    "s_add_u32 m0, s62, 0xc000\n"
    "v_mfma_f32_32x32x16_bf16 v[80:95], a[68:71], v[52:55], v[80:95]\n"
    "ds_read_b128 v[32:35], v11 offset:16384\n"
    "ds_read_b128 v[36:39], v11 offset:17408\n"
    "buffer_load_dwordx4 v16, s[36:39], s59 offen offset:0 sc1 lds\n"
    "s_waitcnt lgkmcnt(4)\n"
    "v_mfma_f32_32x32x16_bf16 v[96:111], a[68:71], v[56:59], v[96:111]\n"
    "ds_read_b128 v[40:43], v11 offset:18432\n"
    "ds_read_b128 v[44:47], v11 offset:19456\n"
    "buffer_load_dwordx4 v17, s[36:39], s59 offen offset:1024 sc1 lds\n"
    "v_mfma_f32_32x32x16_bf16 v[112:127], a[68:71], v[60:63], v[112:127]\n"
    "s_waitcnt lgkmcnt(2)\n"
    "v_mfma_f32_32x32x16_bf16 v[64:79], a[72:75], v[32:35], v[64:79]\n"
    "ds_read_b64_tr_b16 a[76:77], v15 offset:12288\n"
    "ds_read_b64_tr_b16 a[78:79], v15 offset:12800\n"
    "buffer_load_dwordx4 v18, s[36:39], s59 offen offset:2048 sc1 lds\n"
    "v_mfma_f32_32x32x16_bf16 v[80:95], a[72:75], v[36:39], v[80:95]\n"
    "ds_read_b128 v[48:51], v12 offset:16384\n"
    "ds_read_b128 v[52:55], v12 offset:17408\n"
    "s_waitcnt lgkmcnt(4)\n"
    "v_mfma_f32_32x32x16_bf16 v[96:111], a[72:75], v[40:43], v[96:111]\n"
    "ds_read_b128 v[56:59], v12 offset:18432\n"
    "ds_read_b128 v[60:63], v12 offset:19456\n"
    "buffer_load_dwordx4 v19, s[36:39], s59 offen offset:3072 sc1 lds\n"
    "v_mfma_f32_32x32x16_bf16 v[112:127], a[72:75], v[44:47], v[112:127]\n"
    "s_waitcnt lgkmcnt(2)\n"
    "v_mfma_f32_32x32x16_bf16 v[64:79], a[76:79], v[48:51], v[64:79]\n"
    "s_waitcnt lgkmcnt(0)\n"
    "s_waitcnt vmcnt(8)\n"
    "s_barrier\n"
    "ds_read_b64_tr_b16 a[80:81], v13 offset:0\n"
    "ds_read_b64_tr_b16 a[82:83], v13 offset:512\n"
    "v_mfma_f32_32x32x16_bf16 v[80:95], a[76:79], v[52:55], v[80:95]\n"
    "ds_read_b128 v[32:35], v1 offset:16384\n"
    "ds_read_b128 v[36:39], v1 offset:17408\n"
    "s_add_u32 s63, s63, 1\n"
    "s_cmp_eq_u32 s63, 10\n"
    "s_cselect_b32 s63, 0, s63\n"
    "v_mfma_f32_32x32x16_bf16 v[96:111], a[76:79], v[56:59], v[96:111]\n"
    "ds_read_b128 v[40:43], v1 offset:18432\n"
    "ds_read_b128 v[44:47], v1 offset:19456\n"
    "s_mul_i32 s58, s63, 221184\n"
    "s_add_u32 s58, s58, s65\n"
    "s_add_u32 m0, s62, 0x10000\n"
    "v_mfma_f32_32x32x16_bf16 v[112:127], a[76:79], v[60:63], v[112:127]\n"
    "buffer_load_dwordx4 v24, s[40:43], s58 offen offset:0 sc1 lds\n"
    "buffer_load_dwordx4 v25, s[40:43], s58 offen offset:1024 sc1 lds\n"
    "s_waitcnt lgkmcnt(2)\n"
    "v_mfma_f32_32x32x16_bf16 v[64:79], a[80:83], v[32:35], v[64:79]\n"
    "ds_read_b64_tr_b16 a[84:85], v13 offset:4096\n"
    "ds_read_b64_tr_b16 a[86:87], v13 offset:4608\n"
    "buffer_load_dwordx4 v26, s[44:47], s58 offen offset:2048 sc1 lds\n"
    "v_mfma_f32_32x32x16_bf16 v[80:95], a[80:83], v[36:39], v[80:95]\n"
    "ds_read_b128 v[48:51], v2 offset:16384\n"
    "ds_read_b128 v[52:55], v2 offset:17408\n"
    "s_waitcnt lgkmcnt(4)\n"
    "v_mfma_f32_32x32x16_bf16 v[96:111], a[80:83], v[40:43], v[96:111]\n"
    "ds_read_b128 v[56:59], v2 offset:18432\n"
    "ds_read_b128 v[60:63], v2 offset:19456\n"
    "buffer_load_dwordx4 v27, s[44:47], s58 offen offset:3072 sc1 lds\n"
    "v_mfma_f32_32x32x16_bf16 v[112:127], a[80:83], v[44:47], v[112:127]\n"
    "s_waitcnt lgkmcnt(2)\n"
    "v_mfma_f32_32x32x16_bf16 v[64:79], a[84:87], v[48:51], v[64:79]\n"
    "ds_read_b64_tr_b16 a[88:89], v13 offset:8192\n"
    "ds_read_b64_tr_b16 a[90:91], v13 offset:8704\n"
    "s_lshl_b32 s59, s63, 7\n"
    "s_add_u32 s59, s59, s79\n"
    "s_add_u32 m0, s62, 0x14000\n"
    "v_mfma_f32_32x32x16_bf16 v[80:95], a[84:87], v[52:55], v[80:95]\n"
    "ds_read_b128 v[32:35], v3 offset:16384\n"
    "ds_read_b128 v[36:39], v3 offset:17408\n"
    "buffer_load_dwordx4 v16, s[36:39], s59 offen offset:0 sc1 lds\n"
    "s_waitcnt lgkmcnt(4)\n"
    "v_mfma_f32_32x32x16_bf16 v[96:111], a[84:87], v[56:59], v[96:111]\n"
    "ds_read_b128 v[40:43], v3 offset:18432\n"
    "ds_read_b128 v[44:47], v3 offset:19456\n"
    "buffer_load_dwordx4 v17, s[36:39], s59 offen offset:1024 sc1 lds\n"
    "v_mfma_f32_32x32x16_bf16 v[112:127], a[84:87], v[60:63], v[112:127]\n"
    "s_waitcnt lgkmcnt(2)\n"
    "v_mfma_f32_32x32x16_bf16 v[64:79], a[88:91], v[32:35], v[64:79]\n"
    "ds_read_b64_tr_b16 a[92:93], v13 offset:12288\n"
    "ds_read_b64_tr_b16 a[94:95], v13 offset:12800\n"
    "buffer_load_dwordx4 v18, s[36:39], s59 offen offset:2048 sc1 lds\n"
    "v_mfma_f32_32x32x16_bf16 v[80:95], a[88:91], v[36:39], v[80:95]\n"
    "ds_read_b128 v[48:51], v4 offset:16384\n"
    "ds_read_b128 v[52:55], v4 offset:17408\n"
    "s_waitcnt lgkmcnt(4)\n"
    "v_mfma_f32_32x32x16_bf16 v[96:111], a[88:91], v[40:43], v[96:111]\n"
    "ds_read_b128 v[56:59], v4 offset:18432\n"
    "ds_read_b128 v[60:63], v4 offset:19456\n"
    "buffer_load_dwordx4 v19, s[36:39], s59 offen offset:3072 sc1 lds\n"
    "v_mfma_f32_32x32x16_bf16 v[112:127], a[88:91], v[44:47], v[112:127]\n"
    "s_waitcnt lgkmcnt(2)\n"
    "v_mfma_f32_32x32x16_bf16 v[64:79], a[92:95], v[48:51], v[64:79]\n"
    "s_waitcnt lgkmcnt(0)\n"
    "s_waitcnt vmcnt(8)\n"
    "s_barrier\n"
    "ds_read_b64_tr_b16 a[96:97], v13 offset:32768\n"
    "ds_read_b64_tr_b16 a[98:99], v13 offset:33280\n"
    "v_mfma_f32_32x32x16_bf16 v[80:95], a[92:95], v[52:55], v[80:95]\n"
    "ds_read_b128 v[32:35], v1 offset:49152\n"
    "ds_read_b128 v[36:39], v1 offset:50176\n"
    "s_add_u32 s63, s63, 1\n"
    "s_cmp_eq_u32 s63, 10\n"
    "s_cselect_b32 s63, 0, s63\n"
    "v_mfma_f32_32x32x16_bf16 v[96:111], a[92:95], v[56:59], v[96:111]\n"
    "ds_read_b128 v[40:43], v1 offset:51200\n"
    "ds_read_b128 v[44:47], v1 offset:52224\n"
    "s_mul_i32 s58, s63, 221184\n"
    "s_add_u32 s58, s58, s65\n"
    "s_add_u32 m0, s62, 0x18000\n"
    "v_mfma_f32_32x32x16_bf16 v[112:127], a[92:95], v[60:63], v[112:127]\n"
    "buffer_load_dwordx4 v24, s[40:43], s58 offen offset:0 sc1 lds\n"
    "buffer_load_dwordx4 v25, s[40:43], s58 offen offset:1024 sc1 lds\n"
    "s_waitcnt lgkmcnt(2)\n"
    "v_mfma_f32_32x32x16_bf16 v[64:79], a[96:99], v[32:35], v[64:79]\n"
    "ds_read_b64_tr_b16 a[100:101], v13 offset:36864\n"
    "ds_read_b64_tr_b16 a[102:103], v13 offset:37376\n"
    "buffer_load_dwordx4 v26, s[44:47], s58 offen offset:2048 sc1 lds\n"
    "v_mfma_f32_32x32x16_bf16 v[80:95], a[96:99], v[36:39], v[80:95]\n"
    "ds_read_b128 v[48:51], v2 offset:49152\n"
    "ds_read_b128 v[52:55], v2 offset:50176\n"
    "s_waitcnt lgkmcnt(4)\n"
    "v_mfma_f32_32x32x16_bf16 v[96:111], a[96:99], v[40:43], v[96:111]\n"
    "ds_read_b128 v[56:59], v2 offset:51200\n"
    "ds_read_b128 v[60:63], v2 offset:52224\n"
    "buffer_load_dwordx4 v27, s[44:47], s58 offen offset:3072 sc1 lds\n"
    "v_mfma_f32_32x32x16_bf16 v[112:127], a[96:99], v[44:47], v[112:127]\n"
    "s_waitcnt lgkmcnt(2)\n"
    "v_mfma_f32_32x32x16_bf16 v[64:79], a[100:103], v[48:51], v[64:79]\n"
    "ds_read_b64_tr_b16 a[104:105], v13 offset:40960\n"
    "ds_read_b64_tr_b16 a[106:107], v13 offset:41472\n"
    "s_lshl_b32 s59, s63, 7\n"
    "s_add_u32 s59, s59, s79\n"
    "s_add_u32 m0, s62, 0x1c000\n"
    "v_mfma_f32_32x32x16_bf16 v[80:95], a[100:103], v[52:55], v[80:95]\n"
    "ds_read_b128 v[32:35], v3 offset:49152\n"
    "ds_read_b128 v[36:39], v3 offset:50176\n"
    "buffer_load_dwordx4 v16, s[36:39], s59 offen offset:0 sc1 lds\n"
    "s_waitcnt lgkmcnt(4)\n"
    "v_mfma_f32_32x32x16_bf16 v[96:111], a[100:103], v[56:59], v[96:111]\n"
    "ds_read_b128 v[40:43], v3 offset:51200\n"
    "ds_read_b128 v[44:47], v3 offset:52224\n"
    "buffer_load_dwordx4 v17, s[36:39], s59 offen offset:1024 sc1 lds\n"
    "v_mfma_f32_32x32x16_bf16 v[112:127], a[100:103], v[60:63], v[112:127]\n"
    "s_waitcnt lgkmcnt(2)\n"
    "v_mfma_f32_32x32x16_bf16 v[64:79], a[104:107], v[32:35], v[64:79]\n"
    "ds_read_b64_tr_b16 a[108:109], v13 offset:45056\n"
    "ds_read_b64_tr_b16 a[110:111], v13 offset:45568\n"
    "buffer_load_dwordx4 v18, s[36:39], s59 offen offset:2048 sc1 lds\n"
    "v_mfma_f32_32x32x16_bf16 v[80:95], a[104:107], v[36:39], v[80:95]\n"
    "ds_read_b128 v[48:51], v4 offset:49152\n"
    "ds_read_b128 v[52:55], v4 offset:50176\n"
    "s_waitcnt lgkmcnt(4)\n"
    "v_mfma_f32_32x32x16_bf16 v[96:111], a[104:107], v[40:43], v[96:111]\n"
    "ds_read_b128 v[56:59], v4 offset:51200\n"
    "ds_read_b128 v[60:63], v4 offset:52224\n"
    "buffer_load_dwordx4 v19, s[36:39], s59 offen offset:3072 sc1 lds\n"
    "v_mfma_f32_32x32x16_bf16 v[112:127], a[104:107], v[44:47], v[112:127]\n"
    "s_waitcnt lgkmcnt(2)\n"
    "v_mfma_f32_32x32x16_bf16 v[64:79], a[108:111], v[48:51], v[64:79]\n"
    "s_waitcnt lgkmcnt(0)\n"
    "s_waitcnt vmcnt(8)\n"
    "s_barrier\n"
    "ds_read_b64_tr_b16 a[112:113], v14 offset:0\n"
    "ds_read_b64_tr_b16 a[114:115], v14 offset:512\n"
    "v_mfma_f32_32x32x16_bf16 v[80:95], a[108:111], v[52:55], v[80:95]\n"
    "ds_read_b128 v[32:35], v5 offset:16384\n"
    "ds_read_b128 v[36:39], v5 offset:17408\n"
    "s_add_u32 s63, s63, 1\n"
    "s_cmp_eq_u32 s63, 10\n"
    "s_cselect_b32 s63, 0, s63\n"
    "v_mfma_f32_32x32x16_bf16 v[96:111], a[108:111], v[56:59], v[96:111]\n"
    "ds_read_b128 v[40:43], v5 offset:18432\n"
    "ds_read_b128 v[44:47], v5 offset:19456\n"
    "s_mul_i32 s58, s63, 221184\n"
    "s_add_u32 s58, s58, s65\n"
    "s_add_u32 m0, s62, 0x20000\n"
    "v_mfma_f32_32x32x16_bf16 v[112:127], a[108:111], v[60:63], v[112:127]\n"
    "buffer_load_dwordx4 v24, s[40:43], s58 offen offset:0 sc1 lds\n"
    "buffer_load_dwordx4 v25, s[40:43], s58 offen offset:1024 sc1 lds\n"
    "s_waitcnt lgkmcnt(2)\n"
    "v_mfma_f32_32x32x16_bf16 v[64:79], a[112:115], v[32:35], v[64:79]\n"
    "ds_read_b64_tr_b16 a[116:117], v14 offset:4096\n"
    "ds_read_b64_tr_b16 a[118:119], v14 offset:4608\n"
    "buffer_load_dwordx4 v26, s[44:47], s58 offen offset:2048 sc1 lds\n"
    "v_mfma_f32_32x32x16_bf16 v[80:95], a[112:115], v[36:39], v[80:95]\n"
    "ds_read_b128 v[48:51], v6 offset:16384\n"
    "ds_read_b128 v[52:55], v6 offset:17408\n"
    "s_waitcnt lgkmcnt(4)\n"
    "v_mfma_f32_32x32x16_bf16 v[96:111], a[112:115], v[40:43], v[96:111]\n"
    "ds_read_b128 v[56:59], v6 offset:18432\n"
    "ds_read_b128 v[60:63], v6 offset:19456\n"
    "buffer_load_dwordx4 v27, s[44:47], s58 offen offset:3072 sc1 lds\n"
    "v_mfma_f32_32x32x16_bf16 v[112:127], a[112:115], v[44:47], v[112:127]\n"
    "s_waitcnt lgkmcnt(2)\n"
    "v_mfma_f32_32x32x16_bf16 v[64:79], a[116:119], v[48:51], v[64:79]\n"
    "ds_read_b64_tr_b16 a[120:121], v14 offset:8192\n"
    "ds_read_b64_tr_b16 a[122:123], v14 offset:8704\n"
    "s_lshl_b32 s59, s63, 7\n"
    "s_add_u32 s59, s59, s79\n"
    "s_add_u32 m0, s62, 0x24000\n"
    "v_mfma_f32_32x32x16_bf16 v[80:95], a[116:119], v[52:55], v[80:95]\n"
    "ds_read_b128 v[32:35], v7 offset:16384\n"
    "ds_read_b128 v[36:39], v7 offset:17408\n"
    "buffer_load_dwordx4 v16, s[36:39], s59 offen offset:0 sc1 lds\n"
    "s_waitcnt lgkmcnt(4)\n"
    "v_mfma_f32_32x32x16_bf16 v[96:111], a[116:119], v[56:59], v[96:111]\n"
    "ds_read_b128 v[40:43], v7 offset:18432\n"
    "ds_read_b128 v[44:47], v7 offset:19456\n"
    "buffer_load_dwordx4 v17, s[36:39], s59 offen offset:1024 sc1 lds\n"
    "v_mfma_f32_32x32x16_bf16 v[112:127], a[116:119], v[60:63], v[112:127]\n"
    "s_waitcnt lgkmcnt(2)\n"
    "v_mfma_f32_32x32x16_bf16 v[64:79], a[120:123], v[32:35], v[64:79]\n"
    "ds_read_b64_tr_b16 a[124:125], v14 offset:12288\n"
    "ds_read_b64_tr_b16 a[126:127], v14 offset:12800\n"
    "buffer_load_dwordx4 v18, s[36:39], s59 offen offset:2048 sc1 lds\n"
    "v_mfma_f32_32x32x16_bf16 v[80:95], a[120:123], v[36:39], v[80:95]\n"
    "ds_read_b128 v[48:51], v8 offset:16384\n"
    "ds_read_b128 v[52:55], v8 offset:17408\n"
    "s_waitcnt lgkmcnt(4)\n"
    "v_mfma_f32_32x32x16_bf16 v[96:111], a[120:123], v[40:43], v[96:111]\n"
    "ds_read_b128 v[56:59], v8 offset:18432\n"
    "ds_read_b128 v[60:63], v8 offset:19456\n"
    "buffer_load_dwordx4 v19, s[36:39], s59 offen offset:3072 sc1 lds\n"
    "v_mfma_f32_32x32x16_bf16 v[112:127], a[120:123], v[44:47], v[112:127]\n"
    "s_waitcnt lgkmcnt(2)\n"
    "v_mfma_f32_32x32x16_bf16 v[64:79], a[124:127], v[48:51], v[64:79]\n"
    "s_waitcnt lgkmcnt(0)\n"
    "s_waitcnt vmcnt(8)\n"
    "s_barrier\n"
    "ds_read_b64_tr_b16 a[128:129], v14 offset:32768\n"
    "ds_read_b64_tr_b16 a[130:131], v14 offset:33280\n"
    "v_mfma_f32_32x32x16_bf16 v[80:95], a[124:127], v[52:55], v[80:95]\n"
    "ds_read_b128 v[32:35], v5 offset:49152\n"
    "ds_read_b128 v[36:39], v5 offset:50176\n"
    "s_add_u32 s63, s63, 1\n"
    "s_cmp_eq_u32 s63, 10\n"
    "s_cselect_b32 s63, 0, s63\n"
    "s_add_u32 s60, s79, 0x28000\n"
    "v_mfma_f32_32x32x16_bf16 v[96:111], a[124:127], v[56:59], v[96:111]\n"
    "ds_read_b128 v[40:43], v5 offset:51200\n"
    "ds_read_b128 v[44:47], v5 offset:52224\n"
    "s_add_u32 m0, s62, 0x4000\n"
    "s_nop 0\n"
    "buffer_load_dwordx4 v160, s[36:39], s60 offen offset:0 sc1 lds\n"
    "v_mfma_f32_32x32x16_bf16 v[112:127], a[124:127], v[60:63], v[112:127]\n"
    "buffer_load_dwordx4 v161, s[36:39], s60 offen offset:1024 sc1 lds\n"
    "s_waitcnt lgkmcnt(2)\n"
    "v_mfma_f32_32x32x16_bf16 v[64:79], a[128:131], v[32:35], v[64:79]\n"
    "ds_read_b64_tr_b16 a[132:133], v14 offset:36864\n"
    "ds_read_b64_tr_b16 a[134:135], v14 offset:37376\n"
    "buffer_load_dwordx4 v162, s[36:39], s60 offen offset:2048 sc1 lds\n"
    "v_mfma_f32_32x32x16_bf16 v[80:95], a[128:131], v[36:39], v[80:95]\n"
    "ds_read_b128 v[48:51], v6 offset:49152\n"
    "ds_read_b128 v[52:55], v6 offset:50176\n"
    "v_mul_u32_u24 v144, 0x199a, v0\n"
    "v_lshrrev_b32 v144, 16, v144\n"
    "v_mul_u32_u24 v145, 10, v144\n"
    "v_sub_u32 v145, v0, v145\n"
    "s_waitcnt lgkmcnt(4)\n"
    "v_mfma_f32_32x32x16_bf16 v[96:111], a[128:131], v[40:43], v[96:111]\n"
    "ds_read_b128 v[56:59], v6 offset:51200\n"
    "ds_read_b128 v[60:63], v6 offset:52224\n"
    "buffer_load_dwordx4 v163, s[36:39], s60 offen offset:3072 sc1 lds\n"
    "v_mfma_f32_32x32x16_bf16 v[112:127], a[128:131], v[44:47], v[112:127]\n"
    "v_mul_u32_u24 v18, 0x500, v144\n"
    "v_lshl_add_u32 v18, v145, 4, v18\n"
    "v_mov_b32 v146, 0x40000000\n"
    "v_cmp_gt_u32 vcc, 40, v0\n"
    "s_nop 1\n"
    "v_cndmask_b32 v18, v146, v18, vcc\n"
    "s_lshl_b32 s70, s77, 2\n"
    "s_waitcnt lgkmcnt(2)\n"
    "v_mfma_f32_32x32x16_bf16 v[64:79], a[132:135], v[48:51], v[64:79]\n"
    "ds_read_b64_tr_b16 a[136:137], v14 offset:40960\n"
    "ds_read_b64_tr_b16 a[138:139], v14 offset:41472\n"
    "s_add_u32 s60, s79, 0x28000\n"
    "s_add_u32 m0, s62, 0x0\n"
    "v_add_u32 v144, s70, v144\n"
    "v_mfma_f32_32x32x16_bf16 v[80:95], a[132:135], v[52:55], v[80:95]\n"
    "ds_read_b128 v[32:35], v7 offset:49152\n"
    "ds_read_b128 v[36:39], v7 offset:50176\n"
    "buffer_load_dwordx4 v164, s[36:39], s60 offen offset:0 sc1 lds\n"
    "v_min_u32 v144, 15, v144\n"
    "s_waitcnt lgkmcnt(4)\n"
    "v_mfma_f32_32x32x16_bf16 v[96:111], a[132:135], v[56:59], v[96:111]\n"
    "ds_read_b128 v[40:43], v7 offset:51200\n"
    "ds_read_b128 v[44:47], v7 offset:52224\n"
    "buffer_load_dwordx4 v165, s[36:39], s60 offen offset:1024 sc1 lds\n"
    "v_mfma_f32_32x32x16_bf16 v[112:127], a[132:135], v[60:63], v[112:127]\n"
    "v_and_b32 v147, 3, v145\n"
    "v_lshl_add_u32 v144, v147, 2, v144\n"
    "v_and_b32 v144, 15, v144\n"
    "v_lshrrev_b32 v146, 2, v145\n"
    "v_lshlrev_b32 v146, 11, v146\n"
    "v_lshl_add_u32 v146, v147, 8, v146\n"
    "s_waitcnt lgkmcnt(2)\n"
    "v_mfma_f32_32x32x16_bf16 v[64:79], a[136:139], v[32:35], v[64:79]\n"
    "ds_read_b64_tr_b16 a[140:141], v14 offset:45056\n"
    "ds_read_b64_tr_b16 a[142:143], v14 offset:45568\n"
    "buffer_load_dwordx4 v166, s[36:39], s60 offen offset:2048 sc1 lds\n"
    "v_mfma_f32_32x32x16_bf16 v[80:95], a[136:139], v[36:39], v[80:95]\n"
    "ds_read_b128 v[48:51], v8 offset:49152\n"
    "ds_read_b128 v[52:55], v8 offset:50176\n"
    "v_lshl_add_u32 v16, v144, 4, v146\n"
    "v_add_u32 v17, 0x400, v16\n"
    "v_subrev_u32 v147, 8, v145\n"
    "v_lshlrev_b32 v147, 9, v147\n"
    "s_waitcnt lgkmcnt(4)\n"
    "v_mfma_f32_32x32x16_bf16 v[96:111], a[136:139], v[40:43], v[96:111]\n"
    "ds_read_b128 v[56:59], v8 offset:51200\n"
    "ds_read_b128 v[60:63], v8 offset:52224\n"
    "buffer_load_dwordx4 v167, s[36:39], s60 offen offset:3072 sc1 lds\n"
    "v_mfma_f32_32x32x16_bf16 v[112:127], a[136:139], v[44:47], v[112:127]\n"
    "v_lshl_add_u32 v147, v144, 4, v147\n"
    "v_add_u32 v147, 0x1000, v147\n"
    "v_add_u32 v146, 0x100, v147\n"
    "v_cmp_gt_u32 vcc, 8, v145\n"
    "s_nop 1\n"
    "v_cndmask_b32 v16, v147, v16, vcc\n"
    "v_cndmask_b32 v17, v146, v17, vcc\n"
    "s_waitcnt lgkmcnt(2)\n"
    "v_mfma_f32_32x32x16_bf16 v[64:79], a[140:143], v[48:51], v[64:79]\n"
    "s_waitcnt lgkmcnt(0)\n"
    "s_waitcnt vmcnt(8)\n"
    "s_barrier\n"
    "ds_read_b64_tr_b16 a[144:145], v15 offset:0\n"
    "ds_read_b64_tr_b16 a[146:147], v15 offset:512\n"
    "s_add_u32 s60, s79, 0x28000\n"
    "s_add_u32 m0, s62, 0x8000\n"
    "v_mfma_f32_32x32x16_bf16 v[80:95], a[140:143], v[52:55], v[80:95]\n"
    "ds_read_b128 v[32:35], v9 offset:16384\n"
    "ds_read_b128 v[36:39], v9 offset:17408\n"
    "buffer_load_dwordx4 v168, s[36:39], s60 offen offset:0 sc1 lds\n"
    "s_mul_i32 s70, s77, 5120\n"
    "v_mfma_f32_32x32x16_bf16 v[96:111], a[140:143], v[56:59], v[96:111]\n"
    "ds_read_b128 v[40:43], v9 offset:18432\n"
    "ds_read_b128 v[44:47], v9 offset:19456\n"
    "buffer_load_dwordx4 v169, s[36:39], s60 offen offset:1024 sc1 lds\n"
    "v_mfma_f32_32x32x16_bf16 v[112:127], a[140:143], v[60:63], v[112:127]\n"
    "v_lshrrev_b32 v145, 4, v0\n"
    "v_lshl_add_u32 v146, v145, 8, s70\n"
    "v_lshl_add_u32 v147, v145, 2, v0\n"
    "v_and_b32 v147, 15, v147\n"
    "v_lshl_add_u32 v19, v147, 4, v146\n"
    "v_lshrrev_b32 v145, 1, v145\n"
    "s_waitcnt lgkmcnt(2)\n"
    "v_mfma_f32_32x32x16_bf16 v[64:79], a[144:147], v[32:35], v[64:79]\n"
    "ds_read_b64_tr_b16 a[148:149], v15 offset:4096\n"
    "ds_read_b64_tr_b16 a[150:151], v15 offset:4608\n"
    "s_add_u32 s60, s79, 0x32000\n"
    "s_add_u32 m0, s62, 0x8000\n"
    "v_lshl_add_u32 v147, v145, 2, v0\n"
    "v_mfma_f32_32x32x16_bf16 v[80:95], a[144:147], v[36:39], v[80:95]\n"
    "ds_read_b128 v[48:51], v10 offset:16384\n"
    "ds_read_b128 v[52:55], v10 offset:17408\n"
    "buffer_load_dwordx4 v170, s[36:39], s60 offen offset:2048 sc1 lds\n"
    "v_and_b32 v147, 15, v147\n"
    "s_waitcnt lgkmcnt(4)\n"
    "v_mfma_f32_32x32x16_bf16 v[96:111], a[144:147], v[40:43], v[96:111]\n"
    "ds_read_b128 v[56:59], v10 offset:18432\n"
    "ds_read_b128 v[60:63], v10 offset:19456\n"
    "buffer_load_dwordx4 v171, s[36:39], s60 offen offset:3072 sc1 lds\n"
    "v_mfma_f32_32x32x16_bf16 v[112:127], a[144:147], v[44:47], v[112:127]\n"
    "v_lshl_add_u32 v20, v147, 4, v146\n"
    "v_add_u32 v16, 0x1e000, v16\n"
    "v_add_u32 v17, 0x1e000, v17\n"
    "v_add_u32 v19, 0x1e000, v19\n"
    "v_add_u32 v20, 0x1e000, v20\n"
    "s_waitcnt lgkmcnt(2)\n"
    "v_mfma_f32_32x32x16_bf16 v[64:79], a[148:151], v[48:51], v[64:79]\n"
    "ds_read_b64_tr_b16 a[152:153], v15 offset:8192\n"
    "ds_read_b64_tr_b16 a[154:155], v15 offset:8704\n"
    "s_add_u32 s60, s79, 0x32000\n"
    "s_add_u32 m0, s62, 0xc000\n"
    "v_mfma_f32_32x32x16_bf16 v[80:95], a[148:151], v[52:55], v[80:95]\n"
    "ds_read_b128 v[32:35], v11 offset:16384\n"
    "ds_read_b128 v[36:39], v11 offset:17408\n"
    "buffer_load_dwordx4 v160, s[36:39], s60 offen offset:0 sc1 lds\n"
    "s_waitcnt lgkmcnt(4)\n"
    "v_mfma_f32_32x32x16_bf16 v[96:111], a[148:151], v[56:59], v[96:111]\n"
    "ds_read_b128 v[40:43], v11 offset:18432\n"
    "ds_read_b128 v[44:47], v11 offset:19456\n"
    "buffer_load_dwordx4 v161, s[36:39], s60 offen offset:1024 sc1 lds\n"
    "v_mfma_f32_32x32x16_bf16 v[112:127], a[148:151], v[60:63], v[112:127]\n"
    "s_waitcnt lgkmcnt(2)\n"
    "v_mfma_f32_32x32x16_bf16 v[64:79], a[152:155], v[32:35], v[64:79]\n"
    "ds_read_b64_tr_b16 a[156:157], v15 offset:12288\n"
    "ds_read_b64_tr_b16 a[158:159], v15 offset:12800\n"
    "buffer_load_dwordx4 v162, s[36:39], s60 offen offset:2048 sc1 lds\n"
    "v_mfma_f32_32x32x16_bf16 v[80:95], a[152:155], v[36:39], v[80:95]\n"
    "ds_read_b128 v[48:51], v12 offset:16384\n"
    "ds_read_b128 v[52:55], v12 offset:17408\n"
    "s_waitcnt lgkmcnt(4)\n"
    "v_mfma_f32_32x32x16_bf16 v[96:111], a[152:155], v[40:43], v[96:111]\n"
    "ds_read_b128 v[56:59], v12 offset:18432\n"
    "ds_read_b128 v[60:63], v12 offset:19456\n"
    "buffer_load_dwordx4 v163, s[36:39], s60 offen offset:3072 sc1 lds\n"
    "v_mfma_f32_32x32x16_bf16 v[112:127], a[152:155], v[44:47], v[112:127]\n"
    "s_waitcnt lgkmcnt(2)\n"
    "v_mfma_f32_32x32x16_bf16 v[64:79], a[156:159], v[48:51], v[64:79]\n"
    "s_waitcnt lgkmcnt(0)\n"
    "s_waitcnt vmcnt(12)\n"
    "s_barrier\n"
    "ds_read_b128 v[32:35], v1 offset:16384\n"
    "ds_read_b128 v[36:39], v2 offset:16384\n"
    "s_add_u32 s60, s79, 0x32000\n"
    "s_add_u32 m0, s62, 0x10000\n"
    "v_mfma_f32_32x32x16_bf16 v[80:95], a[156:159], v[52:55], v[80:95]\n"
    "ds_read_b128 v[40:43], v3 offset:16384\n"
    "ds_read_b128 v[44:47], v4 offset:16384\n"
    "buffer_load_dwordx4 v164, s[36:39], s60 offen offset:0 sc1 lds\n"
    "v_mfma_f32_32x32x16_bf16 v[96:111], a[156:159], v[56:59], v[96:111]\n"
    "ds_read_b128 v[48:51], v1 offset:17408\n"
    "buffer_load_dwordx4 v165, s[36:39], s60 offen offset:1024 sc1 lds\n"
    "v_mfma_f32_32x32x16_bf16 v[112:127], a[156:159], v[60:63], v[112:127]\n"
    "ds_read_b128 v[52:55], v2 offset:17408\n"
    "buffer_load_dwordx4 v166, s[36:39], s60 offen offset:2048 sc1 lds\n"
    "buffer_load_dwordx4 v167, s[36:39], s60 offen offset:3072 sc1 lds\n"
    "s_add_u32 s60, s79, 0x3c000\n"
    "s_add_u32 m0, s62, 0x14000\n"
    "s_nop 0\n"
    "buffer_load_dwordx4 v160, s[36:39], s60 offen offset:0 sc1 lds\n"
    "buffer_load_dwordx4 v161, s[36:39], s60 offen offset:1024 sc1 lds\n"
    "buffer_load_dwordx4 v162, s[36:39], s60 offen offset:2048 sc1 lds\n"
    "buffer_load_dwordx4 v163, s[36:39], s60 offen offset:3072 sc1 lds\n"
    "v_mov_b32 v24, s56\n"
    "v_mov_b32 v25, s57\n"
    "s_mul_i32 s81, s74, 276480\n"
    "s_mul_i32 s70, s77, 71680\n"
    "s_add_u32 s81, s81, s70\n"
    "s_add_u32 s81, s81, 7340032\n"
    "s_cmp_eq_u32 s77, 3\n"
    "s_cselect_b32 s64, 0x40000000, s81\n"
    "s_lshl_b32 s70, s67, 4\n"
    "s_add_u32 s70, s70, s83\n"
    "s_mul_i32 s80, s70, 55296\n"
    "s_mul_i32 s71, s77, 14336\n"
    "s_add_u32 s80, s80, s71\n"
    "s_add_u32 s80, s80, 262144\n"
    "s_cmp_eq_u32 s77, 3\n"
    "s_cselect_b32 s78, 0x40000000, s80\n"
    "s_lshl_b32 s70, s70, 4\n"
    "s_lshl_b32 s71, s77, 2\n"
    "s_add_u32 s70, s70, s71\n"
    "s_mul_i32 s66, s70, 1280\n"
    "s_mul_i32 s70, s74, 160\n"
    "s_add_u32 s66, s66, s70\n"
    "s_lshl_b32 s76, s67, 12\n"
    "s_add_u32 s76, s76, 65536\n"
    "s_mov_b64 s[52:53], %[out]\n"
    "s_and_b32 s53, s53, 0xffff\n"
    "s_mov_b32 s54, 2621440\n"
    "s_mov_b32 s55, 0x20000\n"
    "s_waitcnt lgkmcnt(2)\n"
    "v_mfma_f32_32x32x16_bf16 v[128:143], a[0:3], v[32:35], 0\n"
    "ds_read_b128 v[56:59], v3 offset:17408\n"
    "v_mfma_f32_32x32x16_bf16 v[128:143], a[4:7], v[36:39], v[128:143]\n"
    "ds_read_b128 v[60:63], v4 offset:17408\n"
    "v_mfma_f32_32x32x16_bf16 v[128:143], a[8:11], v[40:43], v[128:143]\n"
    "ds_read_b128 v[32:35], v1 offset:18432\n"
    "s_add_u32 s60, s79, 0x3c000\n"
    "s_add_u32 m0, s62, 0x18000\n"
    "v_mfma_f32_32x32x16_bf16 v[128:143], a[12:15], v[44:47], v[128:143]\n"
    "ds_read_b128 v[36:39], v2 offset:18432\n"
    "buffer_load_dwordx4 v164, s[36:39], s60 offen offset:0 sc1 lds\n"
    "v_mul_f32 v144, v64, v64\n"
    "v_fma_f32 v144, v144, v30, v31\n"
    "v_mul_f32 v144, v64, v144\n"
    "s_waitcnt lgkmcnt(2)\n"
    "v_mfma_f32_32x32x16_bf16 v[128:143], a[16:19], v[48:51], v[128:143]\n"
    "ds_read_b128 v[40:43], v3 offset:18432\n"
    "buffer_load_dwordx4 v165, s[36:39], s60 offen offset:1024 sc1 lds\n"
    "v_exp_f32 v144, v144\n"
    "v_mul_f32 v146, v64, v72\n"
    "v_mfma_f32_32x32x16_bf16 v[128:143], a[20:23], v[52:55], v[128:143]\n"
    "ds_read_b128 v[44:47], v4 offset:18432\n"
    "v_add_f32 v144, 1.0, v144\n"
    "v_rcp_f32 v144, v144\n"
    "v_mul_f32 v145, v65, v65\n"
    "v_mul_f32 v146, v146, v144\n"
    "v_fma_f32 v145, v145, v30, v31\n"
    "v_mul_f32 v145, v65, v145\n"
    "v_mfma_f32_32x32x16_bf16 v[128:143], a[24:27], v[56:59], v[128:143]\n"
    "ds_read_b128 v[48:51], v1 offset:19456\n"
    "buffer_load_dwordx4 v166, s[36:39], s60 offen offset:2048 sc1 lds\n"
    "v_exp_f32 v145, v145\n"
    "v_mul_f32 v147, v65, v73\n"
    "v_add_f32 v145, 1.0, v145\n"
    "v_mfma_f32_32x32x16_bf16 v[128:143], a[28:31], v[60:63], v[128:143]\n"
    "ds_read_b128 v[52:55], v2 offset:19456\n"
    "v_rcp_f32 v145, v145\n"
    "v_mul_f32 v144, v66, v66\n"
    "v_mul_f32 v147, v147, v145\n"
    "v_cvt_pk_bf16_f32 v148, v146, v147\n"
    "v_fma_f32 v144, v144, v30, v31\n"
    "v_mul_f32 v144, v66, v144\n"
    "s_waitcnt lgkmcnt(2)\n"
    "v_mfma_f32_32x32x16_bf16 v[128:143], a[32:35], v[32:35], v[128:143]\n"
    "ds_read_b128 v[56:59], v3 offset:19456\n"
    "buffer_load_dwordx4 v167, s[36:39], s60 offen offset:3072 sc1 lds\n"
    "v_exp_f32 v144, v144\n"
    "v_mul_f32 v146, v66, v74\n"
    "v_mfma_f32_32x32x16_bf16 v[128:143], a[36:39], v[36:39], v[128:143]\n"
    "s_waitcnt vmcnt(20)\n"
    "s_barrier\n"
    "ds_read_b128 v[60:63], v4 offset:19456\n"
    "v_add_f32 v144, 1.0, v144\n"
    "v_rcp_f32 v144, v144\n"
    "v_mul_f32 v145, v67, v67\n"
    "v_mul_f32 v146, v146, v144\n"
    "v_fma_f32 v145, v145, v30, v31\n"
    "v_mfma_f32_32x32x16_bf16 v[128:143], a[40:43], v[40:43], v[128:143]\n"
    "ds_read_b128 v[32:35], v1 offset:0\n"
    "s_add_u32 s60, s79, 0x3c000\n"
    "s_add_u32 m0, s62, 0x1c000\n"
    "v_mul_f32 v145, v67, v145\n"
    "v_exp_f32 v145, v145\n"
    "v_mul_f32 v147, v67, v75\n"
    "v_add_f32 v145, 1.0, v145\n"
    "v_mfma_f32_32x32x16_bf16 v[128:143], a[44:47], v[44:47], v[128:143]\n"
    "ds_read_b128 v[36:39], v2 offset:0\n"
    "buffer_load_dwordx4 v168, s[36:39], s60 offen offset:0 sc1 lds\n"
    "v_rcp_f32 v145, v145\n"
    "v_mul_f32 v144, v68, v68\n"
    "v_mul_f32 v147, v147, v145\n"
    "s_waitcnt lgkmcnt(2)\n"
    "v_mfma_f32_32x32x16_bf16 v[128:143], a[48:51], v[48:51], v[128:143]\n"
    "ds_read_b128 v[40:43], v3 offset:0\n"
    "buffer_load_dwordx4 v169, s[36:39], s60 offen offset:1024 sc1 lds\n"
    "v_cvt_pk_bf16_f32 v149, v146, v147\n"
    "v_fma_f32 v144, v144, v30, v31\n"
    "v_mul_f32 v144, v68, v144\n"
    "v_mfma_f32_32x32x16_bf16 v[128:143], a[52:55], v[52:55], v[128:143]\n"
    "ds_read_b128 v[44:47], v4 offset:0\n"
    "v_exp_f32 v144, v144\n"
    "v_mul_f32 v146, v68, v76\n"
    "v_add_f32 v144, 1.0, v144\n"
    "v_mfma_f32_32x32x16_bf16 v[128:143], a[56:59], v[56:59], v[128:143]\n"
    "ds_read_b128 v[48:51], v1 offset:1024\n"
    "s_mov_b32 s82, 4096\n"
    "buffer_load_dwordx2 v[156:157], v23, s[48:51], s82 offen offset:0 sc1\n"
    "v_rcp_f32 v144, v144\n"
    "v_mul_f32 v145, v69, v69\n"
    "v_mfma_f32_32x32x16_bf16 v[128:143], a[60:63], v[60:63], v[128:143]\n"
    "ds_read_b128 v[52:55], v2 offset:1024\n"
    "s_add_u32 s60, s79, 0x46000\n"
    "s_add_u32 m0, s62, 0x1c000\n"
    "v_mul_f32 v146, v146, v144\n"
    "v_fma_f32 v145, v145, v30, v31\n"
    "v_mul_f32 v145, v69, v145\n"
    "v_exp_f32 v145, v145\n"
    "s_waitcnt lgkmcnt(2)\n"
    "v_mfma_f32_32x32x16_bf16 v[128:143], a[64:67], v[32:35], v[128:143]\n"
    "ds_read_b128 v[56:59], v3 offset:1024\n"
    "buffer_load_dwordx4 v170, s[36:39], s60 offen offset:2048 sc1 lds\n"
    "v_mul_f32 v147, v69, v77\n"
    "v_add_f32 v145, 1.0, v145\n"
    "v_mfma_f32_32x32x16_bf16 v[128:143], a[68:71], v[36:39], v[128:143]\n"
    "ds_read_b128 v[60:63], v4 offset:1024\n"
    "buffer_load_dwordx4 v171, s[36:39], s60 offen offset:3072 sc1 lds\n"
    "v_rcp_f32 v145, v145\n"
    "v_mul_f32 v144, v70, v70\n"
    "v_mul_f32 v147, v147, v145\n"
    "v_mfma_f32_32x32x16_bf16 v[128:143], a[72:75], v[40:43], v[128:143]\n"
    "ds_read_b128 v[32:35], v1 offset:2048\n"
    "s_add_u32 s60, s79, 0x46000\n"
    "s_add_u32 m0, s62, 0x20000\n"
    "v_cvt_pk_bf16_f32 v150, v146, v147\n"
    "v_fma_f32 v144, v144, v30, v31\n"
    "v_mul_f32 v144, v70, v144\n"
    "v_exp_f32 v144, v144\n"
    "v_mfma_f32_32x32x16_bf16 v[128:143], a[76:79], v[44:47], v[128:143]\n"
    "ds_read_b128 v[36:39], v2 offset:2048\n"
    "buffer_load_dwordx4 v160, s[36:39], s60 offen offset:0 sc1 lds\n"
    "v_mul_f32 v146, v70, v78\n"
    "v_add_f32 v144, 1.0, v144\n"
    "v_rcp_f32 v144, v144\n"
    "s_waitcnt lgkmcnt(2)\n"
    "v_mfma_f32_32x32x16_bf16 v[128:143], a[80:83], v[48:51], v[128:143]\n"
    "ds_read_b128 v[40:43], v3 offset:2048\n"
    "buffer_load_dwordx4 v161, s[36:39], s60 offen offset:1024 sc1 lds\n"
    "v_mul_f32 v145, v71, v71\n"
    "v_mul_f32 v146, v146, v144\n"
    "v_fma_f32 v145, v145, v30, v31\n"
    "v_mfma_f32_32x32x16_bf16 v[128:143], a[84:87], v[52:55], v[128:143]\n"
    "ds_read_b128 v[44:47], v4 offset:2048\n"
    "v_mul_f32 v145, v71, v145\n"
    "v_exp_f32 v145, v145\n"
    "v_mul_f32 v147, v71, v79\n"
    "v_add_f32 v145, 1.0, v145\n"
    "v_mfma_f32_32x32x16_bf16 v[128:143], a[88:91], v[56:59], v[128:143]\n"
    "ds_read_b128 v[48:51], v1 offset:3072\n"
    "buffer_load_dwordx4 v162, s[36:39], s60 offen offset:2048 sc1 lds\n"
    "v_rcp_f32 v145, v145\n"
    "s_add_u32 s72, s61, 0x0\n"
    "v_mul_f32 v147, v147, v145\n"
    "v_mfma_f32_32x32x16_bf16 v[128:143], a[92:95], v[60:63], v[128:143]\n"
    "ds_read_b128 v[52:55], v2 offset:3072\n"
    "v_cvt_pk_bf16_f32 v151, v146, v147\n"
    "buffer_store_dwordx4 v[148:151], v28, s[48:51], s72 offen offset:0 sc1\n"
    "v_mul_f32 v144, v80, v80\n"
    "v_fma_f32 v144, v144, v30, v31\n"
    "s_waitcnt lgkmcnt(2)\n"
    "v_mfma_f32_32x32x16_bf16 v[128:143], a[96:99], v[32:35], v[128:143]\n"
    "ds_read_b128 v[56:59], v3 offset:3072\n"
    "buffer_load_dwordx4 v163, s[36:39], s60 offen offset:3072 sc1 lds\n"
    "v_mul_f32 v144, v80, v144\n"
    "v_exp_f32 v144, v144\n"
    "v_mfma_f32_32x32x16_bf16 v[128:143], a[100:103], v[36:39], v[128:143]\n"
    "s_waitcnt vmcnt(22)\n"
    "s_barrier\n"
    "ds_read_b128 v[60:63], v4 offset:3072\n"
    "v_mul_f32 v146, v80, v88\n"
    "v_add_f32 v144, 1.0, v144\n"
    "v_rcp_f32 v144, v144\n"
    "v_mul_f32 v145, v81, v81\n"
    "v_mul_f32 v146, v146, v144\n"
    "v_mfma_f32_32x32x16_bf16 v[128:143], a[104:107], v[40:43], v[128:143]\n"
    "ds_read_b128 v[32:35], v1 offset:32768\n"
    "s_add_u32 s60, s79, 0x46000\n"
    "s_add_u32 m0, s62, 0x24000\n"
    "v_fma_f32 v145, v145, v30, v31\n"
    "v_mul_f32 v145, v81, v145\n"
    "v_exp_f32 v145, v145\n"
    "v_mul_f32 v147, v81, v89\n"
    "v_mfma_f32_32x32x16_bf16 v[128:143], a[108:111], v[44:47], v[128:143]\n"
    "ds_read_b128 v[36:39], v2 offset:32768\n"
    "buffer_load_dwordx4 v164, s[36:39], s60 offen offset:0 sc1 lds\n"
    "v_add_f32 v145, 1.0, v145\n"
    "v_rcp_f32 v145, v145\n"
    "v_mul_f32 v144, v82, v82\n"
    "s_waitcnt lgkmcnt(2)\n"
    "v_mfma_f32_32x32x16_bf16 v[128:143], a[112:115], v[48:51], v[128:143]\n"
    "ds_read_b128 v[40:43], v3 offset:32768\n"
    "buffer_load_dwordx4 v165, s[36:39], s60 offen offset:1024 sc1 lds\n"
    "v_mul_f32 v147, v147, v145\n"
    "v_cvt_pk_bf16_f32 v152, v146, v147\n"
    "v_fma_f32 v144, v144, v30, v31\n"
    "v_mfma_f32_32x32x16_bf16 v[128:143], a[116:119], v[52:55], v[128:143]\n"
    "ds_read_b128 v[44:47], v4 offset:32768\n"
    "v_mul_f32 v144, v82, v144\n"
    "v_exp_f32 v144, v144\n"
    "v_mul_f32 v146, v82, v90\n"
    "v_add_f32 v144, 1.0, v144\n"
    "v_mfma_f32_32x32x16_bf16 v[128:143], a[120:123], v[56:59], v[128:143]\n"
    "ds_read_b128 v[48:51], v1 offset:33792\n"
    "buffer_load_dwordx4 v166, s[36:39], s60 offen offset:2048 sc1 lds\n"
    "v_rcp_f32 v144, v144\n"
    "v_mul_f32 v145, v83, v83\n"
    "v_mul_f32 v146, v146, v144\n"
    "v_mfma_f32_32x32x16_bf16 v[128:143], a[124:127], v[60:63], v[128:143]\n"
    "ds_read_b128 v[52:55], v2 offset:33792\n"
    "v_fma_f32 v145, v145, v30, v31\n"
    "v_mul_f32 v145, v83, v145\n"
    "v_exp_f32 v145, v145\n"
    "v_mul_f32 v147, v83, v91\n"
    "v_add_f32 v145, 1.0, v145\n"
    "s_waitcnt lgkmcnt(2)\n"
    "v_mfma_f32_32x32x16_bf16 v[128:143], a[128:131], v[32:35], v[128:143]\n"
    "ds_read_b128 v[56:59], v3 offset:33792\n"
    "buffer_load_dwordx4 v167, s[36:39], s60 offen offset:3072 sc1 lds\n"
    "v_rcp_f32 v145, v145\n"
    "v_mul_f32 v144, v84, v84\n"
    "v_mfma_f32_32x32x16_bf16 v[128:143], a[132:135], v[36:39], v[128:143]\n"
    "ds_read_b128 v[60:63], v4 offset:33792\n"
    "v_mul_f32 v147, v147, v145\n"
    "v_cvt_pk_bf16_f32 v153, v146, v147\n"
    "v_fma_f32 v144, v144, v30, v31\n"
    "v_mul_f32 v144, v84, v144\n"
    "v_exp_f32 v144, v144\n"
    "v_mul_f32 v146, v84, v92\n"
    "v_mfma_f32_32x32x16_bf16 v[128:143], a[136:139], v[40:43], v[128:143]\n"
    "ds_read_b128 v[32:35], v1 offset:49152\n"
    "v_add_f32 v144, 1.0, v144\n"
    "v_rcp_f32 v144, v144\n"
    "v_mul_f32 v145, v85, v85\n"
    "v_mul_f32 v146, v146, v144\n"
    "v_fma_f32 v145, v145, v30, v31\n"
    "v_mul_f32 v145, v85, v145\n"
    "v_mfma_f32_32x32x16_bf16 v[128:143], a[140:143], v[44:47], v[128:143]\n"
    "ds_read_b128 v[36:39], v2 offset:49152\n"
    "v_exp_f32 v145, v145\n"
    "v_mul_f32 v147, v85, v93\n"
    "v_add_f32 v145, 1.0, v145\n"
    "s_waitcnt lgkmcnt(2)\n"
    "v_mfma_f32_32x32x16_bf16 v[128:143], a[144:147], v[48:51], v[128:143]\n"
    "ds_read_b128 v[40:43], v3 offset:49152\n"
    "s_waitcnt vmcnt(11)\n"
    "v_cmp_eq_u64 vcc, s[56:57], v[156:157]\n"
    "s_and_b64 s[70:71], vcc, s[98:99]\n"
    "s_cmp_eq_u64 s[70:71], s[98:99]\n"
    "s_cbranch_scc1 L_ok1_%=\n"
    "s_mov_b32 s74, 0x4000\n"
    "L_spin1_%=:\n"
    "s_sleep 2\n"
    "s_mov_b32 s82, 4096\n"
    "buffer_load_dwordx2 v[156:157], v23, s[48:51], s82 offen sc1\n"
    "s_waitcnt vmcnt(0)\n"
    "v_cmp_eq_u64 vcc, s[56:57], v[156:157]\n"
    "s_and_b64 s[70:71], vcc, s[98:99]\n"
    "s_cmp_eq_u64 s[70:71], s[98:99]\n"
    "s_cbranch_scc1 L_ok1_%=\n"
    "s_sub_u32 s74, s74, 1\n"
    "s_cmp_lg_u32 s74, 0\n"
    "s_cbranch_scc1 L_spin1_%=\n"
    "L_ok1_%=:\n"
    "v_rcp_f32 v145, v145\n"
    "v_mfma_f32_32x32x16_bf16 v[128:143], a[148:151], v[52:55], v[128:143]\n"
    "ds_read_b128 v[44:47], v4 offset:49152\n"
    "s_add_u32 s63, s81, 0x0\n"
    "buffer_load_dwordx4 a[160:163], v29, s[48:51], s63 offen offset:0 sc1\n"
    "v_mul_f32 v144, v86, v86\n"
    "v_mul_f32 v147, v147, v145\n"
    "v_cvt_pk_bf16_f32 v154, v146, v147\n"
    "v_mfma_f32_32x32x16_bf16 v[128:143], a[152:155], v[56:59], v[128:143]\n"
    "ds_read_b128 v[48:51], v1 offset:50176\n"
    "buffer_load_dwordx4 a[164:167], v29, s[48:51], s63 offen offset:1024 sc1\n"
    "v_fma_f32 v144, v144, v30, v31\n"
    "v_mul_f32 v144, v86, v144\n"
    "v_exp_f32 v144, v144\n"
    "v_mfma_f32_32x32x16_bf16 v[128:143], a[156:159], v[60:63], v[128:143]\n"
    "ds_read_b128 v[52:55], v2 offset:50176\n"
    "v_mul_f32 v146, v86, v94\n"
    "v_add_f32 v144, 1.0, v144\n"
    "v_rcp_f32 v144, v144\n"
    "v_mul_f32 v145, v87, v87\n"
    "v_mul_f32 v146, v146, v144\n"
    "v_fma_f32 v145, v145, v30, v31\n"
    "s_waitcnt lgkmcnt(2)\n"
    "v_mfma_f32_32x32x16_bf16 v[64:79], a[0:3], v[32:35], 0\n"
    "ds_read_b128 v[56:59], v3 offset:50176\n"
    "buffer_load_dwordx4 a[168:171], v29, s[48:51], s63 offen offset:2048 sc1\n"
    "v_mul_f32 v145, v87, v145\n"
    "v_exp_f32 v145, v145\n"
    "v_mfma_f32_32x32x16_bf16 v[64:79], a[4:7], v[36:39], v[64:79]\n"
    "ds_read_b128 v[60:63], v4 offset:50176\n"
    "v_mul_f32 v147, v87, v95\n"
    "v_add_f32 v145, 1.0, v145\n"
    "v_rcp_f32 v145, v145\n"
    "s_add_u32 s72, s61, 0x1b000\n"
    "v_mul_f32 v147, v147, v145\n"
    "v_cvt_pk_bf16_f32 v155, v146, v147\n"
    "v_mfma_f32_32x32x16_bf16 v[64:79], a[8:11], v[40:43], v[64:79]\n"
    "ds_read_b128 v[32:35], v1 offset:51200\n"
    "buffer_load_dwordx4 a[172:175], v29, s[48:51], s63 offen offset:3072 sc1\n"
    "buffer_store_dwordx4 v[152:155], v28, s[48:51], s72 offen offset:0 sc1\n"
    "v_mfma_f32_32x32x16_bf16 v[64:79], a[12:15], v[44:47], v[64:79]\n"
    "ds_read_b128 v[36:39], v2 offset:51200\n"
    "v_mul_f32 v144, v96, v96\n"
    "v_fma_f32 v144, v144, v30, v31\n"
    "v_mul_f32 v144, v96, v144\n"
    "v_exp_f32 v144, v144\n"
    "v_mul_f32 v146, v96, v104\n"
    "v_add_f32 v144, 1.0, v144\n"
    "s_waitcnt lgkmcnt(2)\n"
    "v_mfma_f32_32x32x16_bf16 v[64:79], a[16:19], v[48:51], v[64:79]\n"
    "ds_read_b128 v[40:43], v3 offset:51200\n"
    "s_add_u32 s63, s81, 0x1000\n"
    "buffer_load_dwordx4 a[176:179], v29, s[48:51], s63 offen offset:0 sc1\n"
    "v_rcp_f32 v144, v144\n"
    "v_mfma_f32_32x32x16_bf16 v[64:79], a[20:23], v[52:55], v[64:79]\n"
    "ds_read_b128 v[44:47], v4 offset:51200\n"
    "v_mul_f32 v145, v97, v97\n"
    "v_mul_f32 v146, v146, v144\n"
    "v_fma_f32 v145, v145, v30, v31\n"
    "v_mul_f32 v145, v97, v145\n"
    "v_exp_f32 v145, v145\n"
    "v_mul_f32 v147, v97, v105\n"
    "v_mfma_f32_32x32x16_bf16 v[64:79], a[24:27], v[56:59], v[64:79]\n"
    "ds_read_b128 v[48:51], v1 offset:52224\n"
    "buffer_load_dwordx4 a[180:183], v29, s[48:51], s63 offen offset:1024 sc1\n"
    "v_add_f32 v145, 1.0, v145\n"
    "v_rcp_f32 v145, v145\n"
    "v_mul_f32 v144, v98, v98\n"
    "v_mfma_f32_32x32x16_bf16 v[64:79], a[28:31], v[60:63], v[64:79]\n"
    "ds_read_b128 v[52:55], v2 offset:52224\n"
    "v_mul_f32 v147, v147, v145\n"
    "v_cvt_pk_bf16_f32 v148, v146, v147\n"
    "v_fma_f32 v144, v144, v30, v31\n"
    "v_mul_f32 v144, v98, v144\n"
    "v_exp_f32 v144, v144\n"
    "v_mul_f32 v146, v98, v106\n"
    "s_waitcnt lgkmcnt(2)\n"
    "v_mfma_f32_32x32x16_bf16 v[64:79], a[32:35], v[32:35], v[64:79]\n"
    "ds_read_b128 v[56:59], v3 offset:52224\n"
    "buffer_load_dwordx4 a[184:187], v29, s[48:51], s63 offen offset:2048 sc1\n"
    "v_add_f32 v144, 1.0, v144\n"
    "v_rcp_f32 v144, v144\n"
    "v_mfma_f32_32x32x16_bf16 v[64:79], a[36:39], v[36:39], v[64:79]\n"
    "s_barrier\n"
    "ds_read_b128 v[60:63], v4 offset:52224\n"
    "v_mul_f32 v145, v99, v99\n"
    "v_mul_f32 v146, v146, v144\n"
    "v_fma_f32 v145, v145, v30, v31\n"
    "v_mul_f32 v145, v99, v145\n"
    "v_exp_f32 v145, v145\n"
    "v_mfma_f32_32x32x16_bf16 v[64:79], a[40:43], v[40:43], v[64:79]\n"
    "ds_read_b128 v[32:35], v5 offset:0\n"
    "buffer_load_dwordx4 a[188:191], v29, s[48:51], s63 offen offset:3072 sc1\n"
    "v_mul_f32 v147, v99, v107\n"
    "v_add_f32 v145, 1.0, v145\n"
    "v_rcp_f32 v145, v145\n"
    "v_mfma_f32_32x32x16_bf16 v[64:79], a[44:47], v[44:47], v[64:79]\n"
    "ds_read_b128 v[36:39], v6 offset:0\n"
    "v_mul_f32 v144, v100, v100\n"
    "v_mul_f32 v147, v147, v145\n"
    "v_cvt_pk_bf16_f32 v149, v146, v147\n"
    "v_fma_f32 v144, v144, v30, v31\n"
    "v_mul_f32 v144, v100, v144\n"
    "v_exp_f32 v144, v144\n"
    "s_waitcnt lgkmcnt(2)\n"
    "v_mfma_f32_32x32x16_bf16 v[64:79], a[48:51], v[48:51], v[64:79]\n"
    "ds_read_b128 v[40:43], v7 offset:0\n"
    "s_add_u32 s63, s81, 0x2000\n"
    "buffer_load_dwordx4 a[192:195], v29, s[48:51], s63 offen offset:0 sc1\n"
    "v_mul_f32 v146, v100, v108\n"
    "v_add_f32 v144, 1.0, v144\n"
    "v_mfma_f32_32x32x16_bf16 v[64:79], a[52:55], v[52:55], v[64:79]\n"
    "ds_read_b128 v[44:47], v8 offset:0\n"
    "v_rcp_f32 v144, v144\n"
    "v_mul_f32 v145, v101, v101\n"
    "v_mul_f32 v146, v146, v144\n"
    "v_fma_f32 v145, v145, v30, v31\n"
    "v_mul_f32 v145, v101, v145\n"
    "v_mfma_f32_32x32x16_bf16 v[64:79], a[56:59], v[56:59], v[64:79]\n"
    "ds_read_b128 v[48:51], v5 offset:1024\n"
    "buffer_load_dwordx4 a[196:199], v29, s[48:51], s63 offen offset:1024 sc1\n"
    "v_exp_f32 v145, v145\n"
    "v_mul_f32 v147, v101, v109\n"
    "v_add_f32 v145, 1.0, v145\n"
    "v_mfma_f32_32x32x16_bf16 v[64:79], a[60:63], v[60:63], v[64:79]\n"
    "ds_read_b128 v[52:55], v6 offset:1024\n"
    "v_rcp_f32 v145, v145\n"
    "v_mul_f32 v144, v102, v102\n"
    "v_mul_f32 v147, v147, v145\n"
    "v_cvt_pk_bf16_f32 v150, v146, v147\n"
    "v_fma_f32 v144, v144, v30, v31\n"
    "v_mul_f32 v144, v102, v144\n"
    "s_waitcnt lgkmcnt(2)\n"
    "v_mfma_f32_32x32x16_bf16 v[64:79], a[64:67], v[32:35], v[64:79]\n"
    "ds_read_b128 v[56:59], v7 offset:1024\n"
    "buffer_load_dwordx4 a[200:203], v29, s[48:51], s63 offen offset:2048 sc1\n"
    "v_exp_f32 v144, v144\n"
    "v_mul_f32 v146, v102, v110\n"
    "v_mfma_f32_32x32x16_bf16 v[64:79], a[68:71], v[36:39], v[64:79]\n"
    "ds_read_b128 v[60:63], v8 offset:1024\n"
    "v_add_f32 v144, 1.0, v144\n"
    "v_rcp_f32 v144, v144\n"
    "v_mul_f32 v145, v103, v103\n"
    "v_mul_f32 v146, v146, v144\n"
    "v_fma_f32 v145, v145, v30, v31\n"
    "v_mul_f32 v145, v103, v145\n"
    "v_mfma_f32_32x32x16_bf16 v[64:79], a[72:75], v[40:43], v[64:79]\n"
    "ds_read_b128 v[32:35], v5 offset:2048\n"
    "buffer_load_dwordx4 a[204:207], v29, s[48:51], s63 offen offset:3072 sc1\n"
    "v_exp_f32 v145, v145\n"
    "v_mul_f32 v147, v103, v111\n"
    "v_add_f32 v145, 1.0, v145\n"
    "v_mfma_f32_32x32x16_bf16 v[64:79], a[76:79], v[44:47], v[64:79]\n"
    "ds_read_b128 v[36:39], v6 offset:2048\n"
    "v_rcp_f32 v145, v145\n"
    "s_add_u32 s72, s61, 0x36000\n"
    "v_mul_f32 v147, v147, v145\n"
    "v_cvt_pk_bf16_f32 v151, v146, v147\n"
    "buffer_store_dwordx4 v[148:151], v28, s[48:51], s72 offen offset:0 sc1\n"
    "s_waitcnt lgkmcnt(2)\n"
    "v_mfma_f32_32x32x16_bf16 v[64:79], a[80:83], v[48:51], v[64:79]\n"
    "ds_read_b128 v[40:43], v7 offset:2048\n"
    "s_add_u32 s63, s81, 0x3000\n"
    "buffer_load_dwordx4 a[208:211], v29, s[48:51], s63 offen offset:0 sc1\n"
    "v_mul_f32 v144, v112, v112\n"
    "v_fma_f32 v144, v144, v30, v31\n"
    "v_mfma_f32_32x32x16_bf16 v[64:79], a[84:87], v[52:55], v[64:79]\n"
    "ds_read_b128 v[44:47], v8 offset:2048\n"
    "v_mul_f32 v144, v112, v144\n"
    "v_exp_f32 v144, v144\n"
    "v_mul_f32 v146, v112, v120\n"
    "v_add_f32 v144, 1.0, v144\n"
    "v_mfma_f32_32x32x16_bf16 v[64:79], a[88:91], v[56:59], v[64:79]\n"
    "ds_read_b128 v[48:51], v5 offset:3072\n"
    "buffer_load_dwordx4 a[212:215], v29, s[48:51], s63 offen offset:1024 sc1\n"
    "v_rcp_f32 v144, v144\n"
    "v_mul_f32 v145, v113, v113\n"
    "v_mul_f32 v146, v146, v144\n"
    "v_mfma_f32_32x32x16_bf16 v[64:79], a[92:95], v[60:63], v[64:79]\n"
    "ds_read_b128 v[52:55], v6 offset:3072\n"
    "v_fma_f32 v145, v145, v30, v31\n"
    "v_mul_f32 v145, v113, v145\n"
    "v_exp_f32 v145, v145\n"
    "v_mul_f32 v147, v113, v121\n"
    "v_add_f32 v145, 1.0, v145\n"
    "s_waitcnt lgkmcnt(2)\n"
    "v_mfma_f32_32x32x16_bf16 v[64:79], a[96:99], v[32:35], v[64:79]\n"
    "ds_read_b128 v[56:59], v7 offset:3072\n"
    "buffer_load_dwordx4 a[216:219], v29, s[48:51], s63 offen offset:2048 sc1\n"
    "v_rcp_f32 v145, v145\n"
    "v_mul_f32 v144, v114, v114\n"
    "v_mfma_f32_32x32x16_bf16 v[64:79], a[100:103], v[36:39], v[64:79]\n"
    "ds_read_b128 v[60:63], v8 offset:3072\n"
    "v_mul_f32 v147, v147, v145\n"
    "v_cvt_pk_bf16_f32 v152, v146, v147\n"
    "v_fma_f32 v144, v144, v30, v31\n"
    "v_mul_f32 v144, v114, v144\n"
    "v_exp_f32 v144, v144\n"
    "v_mul_f32 v146, v114, v122\n"
    "v_mfma_f32_32x32x16_bf16 v[64:79], a[104:107], v[40:43], v[64:79]\n"
    "ds_read_b128 v[32:35], v1 offset:34816\n"
    "buffer_load_dwordx4 a[220:223], v29, s[48:51], s63 offen offset:3072 sc1\n"
    "v_add_f32 v144, 1.0, v144\n"
    "v_rcp_f32 v144, v144\n"
    "v_mul_f32 v145, v115, v115\n"
    "v_mfma_f32_32x32x16_bf16 v[64:79], a[108:111], v[44:47], v[64:79]\n"
    "ds_read_b128 v[36:39], v2 offset:34816\n"
    "v_mul_f32 v146, v146, v144\n"
    "v_fma_f32 v145, v145, v30, v31\n"
    "v_mul_f32 v145, v115, v145\n"
    "v_exp_f32 v145, v145\n"
    "v_mul_f32 v147, v115, v123\n"
    "v_add_f32 v145, 1.0, v145\n"
    "s_waitcnt lgkmcnt(2)\n"
    "v_mfma_f32_32x32x16_bf16 v[64:79], a[112:115], v[48:51], v[64:79]\n"
    "ds_read_b128 v[40:43], v3 offset:34816\n"
    "s_add_u32 s63, s81, 0x4000\n"
    "buffer_load_dwordx4 a[224:227], v29, s[48:51], s63 offen offset:0 sc1\n"
    "v_rcp_f32 v145, v145\n"
    "v_mfma_f32_32x32x16_bf16 v[64:79], a[116:119], v[52:55], v[64:79]\n"
    "ds_read_b128 v[44:47], v4 offset:34816\n"
    "v_mul_f32 v144, v116, v116\n"
    "v_mul_f32 v147, v147, v145\n"
    "v_cvt_pk_bf16_f32 v153, v146, v147\n"
    "v_fma_f32 v144, v144, v30, v31\n"
    "v_mul_f32 v144, v116, v144\n"
    "v_exp_f32 v144, v144\n"
    "v_mfma_f32_32x32x16_bf16 v[64:79], a[120:123], v[56:59], v[64:79]\n"
    "ds_read_b128 v[48:51], v1 offset:35840\n"
    "s_waitcnt vmcnt(14)\n"
    "s_barrier\n"
    "s_add_u32 s82, s75, 0\n"
    "buffer_store_dwordx2 v[24:25], v158, s[48:51], s82 offen offset:0 sc1\n"
    "v_mul_f32 v146, v116, v124\n"
    "v_mfma_f32_32x32x16_bf16 v[64:79], a[124:127], v[60:63], v[64:79]\n"
    "ds_read_b128 v[52:55], v2 offset:35840\n"
    "buffer_load_dwordx4 a[228:231], v29, s[48:51], s63 offen offset:1024 sc1\n"
    "v_add_f32 v144, 1.0, v144\n"
    "v_rcp_f32 v144, v144\n"
    "v_mul_f32 v145, v117, v117\n"
    "s_waitcnt lgkmcnt(2)\n"
    "v_mfma_f32_32x32x16_bf16 v[64:79], a[128:131], v[32:35], v[64:79]\n"
    "ds_read_b128 v[56:59], v3 offset:35840\n"
    "buffer_load_dwordx4 a[232:235], v29, s[48:51], s63 offen offset:2048 sc1\n"
    "v_mul_f32 v146, v146, v144\n"
    "v_fma_f32 v145, v145, v30, v31\n"
    "v_mul_f32 v145, v117, v145\n"
    "v_mfma_f32_32x32x16_bf16 v[64:79], a[132:135], v[36:39], v[64:79]\n"
    "s_barrier\n"
    "ds_read_b128 v[60:63], v4 offset:35840\n"
    "v_exp_f32 v145, v145\n"
    "v_mul_f32 v147, v117, v125\n"
    "v_add_f32 v145, 1.0, v145\n"
    "v_mfma_f32_32x32x16_bf16 v[64:79], a[136:139], v[40:43], v[64:79]\n"
    "ds_read_b128 v[32:35], v5 offset:16384\n"
    "buffer_load_dwordx4 a[236:239], v29, s[48:51], s63 offen offset:3072 sc1\n"
    "v_rcp_f32 v145, v145\n"
    "v_mul_f32 v144, v118, v118\n"
    "v_mul_f32 v147, v147, v145\n"
    "v_mfma_f32_32x32x16_bf16 v[64:79], a[140:143], v[44:47], v[64:79]\n"
    "ds_read_b128 v[36:39], v6 offset:16384\n"
    "v_cvt_pk_bf16_f32 v154, v146, v147\n"
    "v_fma_f32 v144, v144, v30, v31\n"
    "v_mul_f32 v144, v118, v144\n"
    "v_exp_f32 v144, v144\n"
    "v_mul_f32 v146, v118, v126\n"
    "v_add_f32 v144, 1.0, v144\n"
    "s_waitcnt lgkmcnt(2)\n"
    "v_mfma_f32_32x32x16_bf16 v[64:79], a[144:147], v[48:51], v[64:79]\n"
    "ds_read_b128 v[40:43], v7 offset:16384\n"
    "s_add_u32 s63, s81, 0x5000\n"
    "buffer_load_dwordx4 a[240:243], v29, s[48:51], s63 offen offset:0 sc1\n"
    "v_rcp_f32 v144, v144\n"
    "v_mfma_f32_32x32x16_bf16 v[64:79], a[148:151], v[52:55], v[64:79]\n"
    "ds_read_b128 v[44:47], v8 offset:16384\n"
    "v_mul_f32 v145, v119, v119\n"
    "v_mul_f32 v146, v146, v144\n"
    "v_fma_f32 v145, v145, v30, v31\n"
    "v_mul_f32 v145, v119, v145\n"
    "v_exp_f32 v145, v145\n"
    "v_mul_f32 v147, v119, v127\n"
    "v_mfma_f32_32x32x16_bf16 v[64:79], a[152:155], v[56:59], v[64:79]\n"
    "ds_read_b128 v[48:51], v5 offset:17408\n"
    "buffer_load_dwordx4 a[244:247], v29, s[48:51], s63 offen offset:1024 sc1\n"
    "v_add_f32 v145, 1.0, v145\n"
    "v_rcp_f32 v145, v145\n"
    "s_add_u32 s72, s61, 0x51000\n"
    "v_mfma_f32_32x32x16_bf16 v[64:79], a[156:159], v[60:63], v[64:79]\n"
    "ds_read_b128 v[52:55], v6 offset:17408\n"
    "v_mul_f32 v147, v147, v145\n"
    "v_cvt_pk_bf16_f32 v155, v146, v147\n"
    "buffer_store_dwordx4 v[152:155], v28, s[48:51], s72 offen offset:0 sc1\n"
    "v_mul_f32 v144, v128, v128\n"
    "s_waitcnt lgkmcnt(2)\n"
    "v_mfma_f32_32x32x16_bf16 v[80:95], a[0:3], v[32:35], 0\n"
    "ds_read_b128 v[56:59], v7 offset:17408\n"
    "buffer_load_dwordx4 a[248:251], v29, s[48:51], s63 offen offset:2048 sc1\n"
    "v_fma_f32 v144, v144, v30, v31\n"
    "v_mul_f32 v144, v128, v144\n"
    "v_mfma_f32_32x32x16_bf16 v[80:95], a[4:7], v[36:39], v[80:95]\n"
    "ds_read_b128 v[60:63], v8 offset:17408\n"
    "v_exp_f32 v144, v144\n"
    "v_mul_f32 v146, v128, v136\n"
    "v_add_f32 v144, 1.0, v144\n"
    "v_mfma_f32_32x32x16_bf16 v[80:95], a[8:11], v[40:43], v[80:95]\n"
    "ds_read_b128 v[32:35], v5 offset:18432\n"
    "buffer_load_dwordx4 a[252:255], v29, s[48:51], s63 offen offset:3072 sc1\n"
    "v_rcp_f32 v144, v144\n"
    "v_mul_f32 v145, v129, v129\n"
    "v_mul_f32 v146, v146, v144\n"
    "v_mfma_f32_32x32x16_bf16 v[80:95], a[12:15], v[44:47], v[80:95]\n"
    "ds_read_b128 v[36:39], v6 offset:18432\n"
    "v_fma_f32 v145, v145, v30, v31\n"
    "v_mul_f32 v145, v129, v145\n"
    "v_exp_f32 v145, v145\n"
    "v_mul_f32 v147, v129, v137\n"
    "v_add_f32 v145, 1.0, v145\n"
    "s_waitcnt lgkmcnt(2)\n"
    "v_mfma_f32_32x32x16_bf16 v[80:95], a[16:19], v[48:51], v[80:95]\n"
    "ds_read_b128 v[40:43], v7 offset:18432\n"
    "s_add_u32 s63, s81, 0x6000\n"
    "buffer_load_dwordx4 v[160:163], v29, s[48:51], s63 offen offset:0 sc1\n"
    "v_rcp_f32 v145, v145\n"
    "v_mfma_f32_32x32x16_bf16 v[80:95], a[20:23], v[52:55], v[80:95]\n"
    "ds_read_b128 v[44:47], v8 offset:18432\n"
    "v_mul_f32 v144, v130, v130\n"
    "v_mul_f32 v147, v147, v145\n"
    "v_cvt_pk_bf16_f32 v148, v146, v147\n"
    "v_fma_f32 v144, v144, v30, v31\n"
    "v_mul_f32 v144, v130, v144\n"
    "v_exp_f32 v144, v144\n"
    "v_mfma_f32_32x32x16_bf16 v[80:95], a[24:27], v[56:59], v[80:95]\n"
    "ds_read_b128 v[48:51], v5 offset:19456\n"
    "buffer_load_dwordx4 v[164:167], v29, s[48:51], s63 offen offset:1024 sc1\n"
    "v_mul_f32 v146, v130, v138\n"
    "v_add_f32 v144, 1.0, v144\n"
    "v_rcp_f32 v144, v144\n"
    "v_mfma_f32_32x32x16_bf16 v[80:95], a[28:31], v[60:63], v[80:95]\n"
    "ds_read_b128 v[52:55], v6 offset:19456\n"
    "v_mul_f32 v145, v131, v131\n"
    "v_mul_f32 v146, v146, v144\n"
    "v_fma_f32 v145, v145, v30, v31\n"
    "v_mul_f32 v145, v131, v145\n"
    "v_exp_f32 v145, v145\n"
    "v_mul_f32 v147, v131, v139\n"
    "s_waitcnt lgkmcnt(2)\n"
    "v_mfma_f32_32x32x16_bf16 v[80:95], a[32:35], v[32:35], v[80:95]\n"
    "ds_read_b128 v[56:59], v7 offset:19456\n"
    "buffer_load_dwordx4 v[168:171], v29, s[48:51], s63 offen offset:2048 sc1\n"
    "v_add_f32 v145, 1.0, v145\n"
    "v_rcp_f32 v145, v145\n"
    "v_mfma_f32_32x32x16_bf16 v[80:95], a[36:39], v[36:39], v[80:95]\n"
    "ds_read_b128 v[60:63], v8 offset:19456\n"
    "v_mul_f32 v144, v132, v132\n"
    "v_mul_f32 v147, v147, v145\n"
    "v_cvt_pk_bf16_f32 v149, v146, v147\n"
    "v_fma_f32 v144, v144, v30, v31\n"
    "v_mul_f32 v144, v132, v144\n"
    "v_exp_f32 v144, v144\n"
    "v_mfma_f32_32x32x16_bf16 v[80:95], a[40:43], v[40:43], v[80:95]\n"
    "ds_read_b128 v[32:35], v5 offset:32768\n"
    "buffer_load_dwordx4 v[172:175], v29, s[48:51], s63 offen offset:3072 sc1\n"
    "v_mul_f32 v146, v132, v140\n"
    "v_add_f32 v144, 1.0, v144\n"
    "v_rcp_f32 v144, v144\n"
    "v_mfma_f32_32x32x16_bf16 v[80:95], a[44:47], v[44:47], v[80:95]\n"
    "ds_read_b128 v[36:39], v6 offset:32768\n"
    "v_mul_f32 v145, v133, v133\n"
    "v_mul_f32 v146, v146, v144\n"
    "v_fma_f32 v145, v145, v30, v31\n"
    "v_mul_f32 v145, v133, v145\n"
    "v_exp_f32 v145, v145\n"
    "v_mul_f32 v147, v133, v141\n"
    "s_waitcnt lgkmcnt(2)\n"
    "v_mfma_f32_32x32x16_bf16 v[80:95], a[48:51], v[48:51], v[80:95]\n"
    "ds_read_b128 v[40:43], v7 offset:32768\n"
    "s_add_u32 s63, s81, 0x7000\n"
    "buffer_load_dwordx4 v[176:179], v29, s[48:51], s63 offen offset:0 sc1\n"
    "v_add_f32 v145, 1.0, v145\n"
    "v_mfma_f32_32x32x16_bf16 v[80:95], a[52:55], v[52:55], v[80:95]\n"
    "ds_read_b128 v[44:47], v8 offset:32768\n"
    "v_rcp_f32 v145, v145\n"
    "v_mul_f32 v144, v134, v134\n"
    "v_mul_f32 v147, v147, v145\n"
    "v_cvt_pk_bf16_f32 v150, v146, v147\n"
    "v_fma_f32 v144, v144, v30, v31\n"
    "v_mul_f32 v144, v134, v144\n"
    "v_mfma_f32_32x32x16_bf16 v[80:95], a[56:59], v[56:59], v[80:95]\n"
    "ds_read_b128 v[48:51], v5 offset:33792\n"
    "buffer_load_dwordx4 v[180:183], v29, s[48:51], s63 offen offset:1024 sc1\n"
    "v_exp_f32 v144, v144\n"
    "v_mul_f32 v146, v134, v142\n"
    "v_add_f32 v144, 1.0, v144\n"
    "v_mfma_f32_32x32x16_bf16 v[80:95], a[60:63], v[60:63], v[80:95]\n"
    "ds_read_b128 v[52:55], v6 offset:33792\n"
    "v_rcp_f32 v144, v144\n"
    "v_mul_f32 v145, v135, v135\n"
    "v_mul_f32 v146, v146, v144\n"
    "v_fma_f32 v145, v145, v30, v31\n"
    "v_mul_f32 v145, v135, v145\n"
    "s_waitcnt lgkmcnt(2)\n"
    "v_mfma_f32_32x32x16_bf16 v[80:95], a[64:67], v[32:35], v[80:95]\n"
    "ds_read_b128 v[56:59], v7 offset:33792\n"
    "buffer_load_dwordx4 v[184:187], v29, s[48:51], s63 offen offset:2048 sc1\n"
    "v_exp_f32 v145, v145\n"
    "v_mul_f32 v147, v135, v143\n"
    "v_mfma_f32_32x32x16_bf16 v[80:95], a[68:71], v[36:39], v[80:95]\n"
    "ds_read_b128 v[60:63], v8 offset:33792\n"
    "v_add_f32 v145, 1.0, v145\n"
    "v_rcp_f32 v145, v145\n"
    "s_add_u32 s72, s61, 0x6c000\n"
    "v_mul_f32 v147, v147, v145\n"
    "v_cvt_pk_bf16_f32 v151, v146, v147\n"
    "v_mfma_f32_32x32x16_bf16 v[80:95], a[72:75], v[40:43], v[80:95]\n"
    "ds_read_b128 v[32:35], v5 offset:34816\n"
    "buffer_load_dwordx4 v[188:191], v29, s[48:51], s63 offen offset:3072 sc1\n"
    "buffer_store_dwordx4 v[148:151], v28, s[48:51], s72 offen offset:0 sc1\n"
    "v_mfma_f32_32x32x16_bf16 v[80:95], a[76:79], v[44:47], v[80:95]\n"
    "ds_read_b128 v[36:39], v6 offset:34816\n"
    "v_mul_f32 v144, v64, v64\n"
    "v_fma_f32 v144, v144, v30, v31\n"
    "v_mul_f32 v144, v64, v144\n"
    "v_exp_f32 v144, v144\n"
    "v_mul_f32 v146, v64, v72\n"
    "v_add_f32 v144, 1.0, v144\n"
    "s_waitcnt lgkmcnt(2)\n"
    "v_mfma_f32_32x32x16_bf16 v[80:95], a[80:83], v[48:51], v[80:95]\n"
    "ds_read_b128 v[40:43], v7 offset:34816\n"
    "s_add_u32 s63, s81, 0x8000\n"
    "buffer_load_dwordx4 v[192:195], v29, s[48:51], s63 offen offset:0 sc1\n"
    "v_rcp_f32 v144, v144\n"
    "v_mfma_f32_32x32x16_bf16 v[80:95], a[84:87], v[52:55], v[80:95]\n"
    "ds_read_b128 v[44:47], v8 offset:34816\n"
    "v_mul_f32 v145, v65, v65\n"
    "v_mul_f32 v146, v146, v144\n"
    "v_fma_f32 v145, v145, v30, v31\n"
    "v_mul_f32 v145, v65, v145\n"
    "v_exp_f32 v145, v145\n"
    "v_mul_f32 v147, v65, v73\n"
    "v_mfma_f32_32x32x16_bf16 v[80:95], a[88:91], v[56:59], v[80:95]\n"
    "ds_read_b128 v[48:51], v5 offset:35840\n"
    "buffer_load_dwordx4 v[196:199], v29, s[48:51], s63 offen offset:1024 sc1\n"
    "v_add_f32 v145, 1.0, v145\n"
    "v_rcp_f32 v145, v145\n"
    "v_mul_f32 v144, v66, v66\n"
    "v_mfma_f32_32x32x16_bf16 v[80:95], a[92:95], v[60:63], v[80:95]\n"
    "ds_read_b128 v[52:55], v6 offset:35840\n"
    "v_mul_f32 v147, v147, v145\n"
    "v_cvt_pk_bf16_f32 v152, v146, v147\n"
    "v_fma_f32 v144, v144, v30, v31\n"
    "v_mul_f32 v144, v66, v144\n"
    "v_exp_f32 v144, v144\n"
    "v_mul_f32 v146, v66, v74\n"
    "s_waitcnt lgkmcnt(2)\n"
    "v_mfma_f32_32x32x16_bf16 v[80:95], a[96:99], v[32:35], v[80:95]\n"
    "ds_read_b128 v[56:59], v7 offset:35840\n"
    "buffer_load_dwordx4 v[200:203], v29, s[48:51], s63 offen offset:2048 sc1\n"
    "v_add_f32 v144, 1.0, v144\n"
    "v_rcp_f32 v144, v144\n"
    "v_mfma_f32_32x32x16_bf16 v[80:95], a[100:103], v[36:39], v[80:95]\n"
    "ds_read_b128 v[60:63], v8 offset:35840\n"
    "v_mul_f32 v145, v67, v67\n"
    "v_mul_f32 v146, v146, v144\n"
    "v_fma_f32 v145, v145, v30, v31\n"
    "v_mul_f32 v145, v67, v145\n"
    "v_exp_f32 v145, v145\n"
    "v_mul_f32 v147, v67, v75\n"
    "v_mfma_f32_32x32x16_bf16 v[80:95], a[104:107], v[40:43], v[80:95]\n"
    "ds_read_b128 v[32:35], v5 offset:49152\n"
    "buffer_load_dwordx4 v[204:207], v29, s[48:51], s63 offen offset:3072 sc1\n"
    "v_add_f32 v145, 1.0, v145\n"
    "v_rcp_f32 v145, v145\n"
    "v_mul_f32 v144, v68, v68\n"
    "v_mfma_f32_32x32x16_bf16 v[80:95], a[108:111], v[44:47], v[80:95]\n"
    "ds_read_b128 v[36:39], v6 offset:49152\n"
    "s_waitcnt vmcnt(15)\n"
    "s_barrier\n"
    "s_add_u32 s82, s75, 32768\n"
    "buffer_store_dwordx2 v[24:25], v158, s[48:51], s82 offen offset:0 sc1\n"
    "v_mul_f32 v147, v147, v145\n"
    "s_waitcnt lgkmcnt(2)\n"
    "v_mfma_f32_32x32x16_bf16 v[80:95], a[112:115], v[48:51], v[80:95]\n"
    "ds_read_b128 v[40:43], v7 offset:49152\n"
    "s_add_u32 s63, s81, 0x9000\n"
    "buffer_load_dwordx4 v[208:211], v29, s[48:51], s63 offen offset:0 sc1\n"
    "v_cvt_pk_bf16_f32 v153, v146, v147\n"
    "v_fma_f32 v144, v144, v30, v31\n"
    "v_mfma_f32_32x32x16_bf16 v[80:95], a[116:119], v[52:55], v[80:95]\n"
    "ds_read_b128 v[44:47], v8 offset:49152\n"
    "v_mul_f32 v144, v68, v144\n"
    "v_exp_f32 v144, v144\n"
    "v_mul_f32 v146, v68, v76\n"
    "v_add_f32 v144, 1.0, v144\n"
    "v_mfma_f32_32x32x16_bf16 v[80:95], a[120:123], v[56:59], v[80:95]\n"
    "ds_read_b128 v[48:51], v5 offset:50176\n"
    "buffer_load_dwordx4 v[212:215], v29, s[48:51], s63 offen offset:1024 sc1\n"
    "v_rcp_f32 v144, v144\n"
    "v_mul_f32 v145, v69, v69\n"
    "v_mul_f32 v146, v146, v144\n"
    "v_mfma_f32_32x32x16_bf16 v[80:95], a[124:127], v[60:63], v[80:95]\n"
    "ds_read_b128 v[52:55], v6 offset:50176\n"
    "v_fma_f32 v145, v145, v30, v31\n"
    "v_mul_f32 v145, v69, v145\n"
    "v_exp_f32 v145, v145\n"
    "v_mul_f32 v147, v69, v77\n"
    "v_add_f32 v145, 1.0, v145\n"
    "s_waitcnt lgkmcnt(2)\n"
    "v_mfma_f32_32x32x16_bf16 v[80:95], a[128:131], v[32:35], v[80:95]\n"
    "ds_read_b128 v[56:59], v7 offset:50176\n"
    "buffer_load_dwordx4 v[216:219], v29, s[48:51], s63 offen offset:2048 sc1\n"
    "v_rcp_f32 v145, v145\n"
    "v_mul_f32 v144, v70, v70\n"
    "v_mfma_f32_32x32x16_bf16 v[80:95], a[132:135], v[36:39], v[80:95]\n"
    "ds_read_b128 v[60:63], v8 offset:50176\n"
    "v_mul_f32 v147, v147, v145\n"
    "v_cvt_pk_bf16_f32 v154, v146, v147\n"
    "v_fma_f32 v144, v144, v30, v31\n"
    "v_mul_f32 v144, v70, v144\n"
    "v_exp_f32 v144, v144\n"
    "v_mul_f32 v146, v70, v78\n"
    "v_mfma_f32_32x32x16_bf16 v[80:95], a[136:139], v[40:43], v[80:95]\n"
    "ds_read_b128 v[32:35], v9 offset:0\n"
    "buffer_load_dwordx4 v[220:223], v29, s[48:51], s63 offen offset:3072 sc1\n"
    "v_add_f32 v144, 1.0, v144\n"
    "v_rcp_f32 v144, v144\n"
    "v_mul_f32 v145, v71, v71\n"
    "v_mfma_f32_32x32x16_bf16 v[80:95], a[140:143], v[44:47], v[80:95]\n"
    "ds_read_b128 v[36:39], v10 offset:0\n"
    "v_mul_f32 v146, v146, v144\n"
    "v_fma_f32 v145, v145, v30, v31\n"
    "v_mul_f32 v145, v71, v145\n"
    "v_exp_f32 v145, v145\n"
    "v_mul_f32 v147, v71, v79\n"
    "v_add_f32 v145, 1.0, v145\n"
    "s_waitcnt lgkmcnt(2)\n"
    "v_mfma_f32_32x32x16_bf16 v[80:95], a[144:147], v[48:51], v[80:95]\n"
    "ds_read_b128 v[40:43], v11 offset:0\n"
    "s_add_u32 s82, s76, 0\n"
    "buffer_load_dwordx2 v[156:157], v159, s[48:51], s82 offen offset:0 sc1\n"
    "v_rcp_f32 v145, v145\n"
    "v_mfma_f32_32x32x16_bf16 v[80:95], a[148:151], v[52:55], v[80:95]\n"
    "ds_read_b128 v[44:47], v12 offset:0\n"
    "s_add_u32 s63, s81, 0xa000\n"
    "buffer_load_dwordx4 v[224:227], v29, s[48:51], s63 offen offset:0 sc1\n"
    "s_add_u32 s72, s61, 0x87000\n"
    "v_mul_f32 v147, v147, v145\n"
    "v_cvt_pk_bf16_f32 v155, v146, v147\n"
    "v_mfma_f32_32x32x16_bf16 v[80:95], a[152:155], v[56:59], v[80:95]\n"
    "ds_read_b128 v[48:51], v9 offset:1024\n"
    "buffer_load_dwordx4 v[228:231], v29, s[48:51], s63 offen offset:1024 sc1\n"
    "buffer_store_dwordx4 v[152:155], v28, s[48:51], s72 offen offset:0 sc1\n"
    "v_mfma_f32_32x32x16_bf16 v[80:95], a[156:159], v[60:63], v[80:95]\n"
    "ds_read_b128 v[52:55], v10 offset:1024\n"
    "s_waitcnt lgkmcnt(2)\n"
    "v_mfma_f32_32x32x16_bf16 v[124:139], a[0:3], v[32:35], 0\n"
    "ds_read_b128 v[56:59], v11 offset:1024\n"
    "buffer_load_dwordx4 v[232:235], v29, s[48:51], s63 offen offset:2048 sc1\n"
    "v_mfma_f32_32x32x16_bf16 v[124:139], a[4:7], v[36:39], v[124:139]\n"
    "ds_read_b128 v[60:63], v12 offset:1024\n"
    "v_mfma_f32_32x32x16_bf16 v[124:139], a[8:11], v[40:43], v[124:139]\n"
    "ds_read_b128 v[32:35], v9 offset:2048\n"
    "buffer_load_dwordx4 v[236:239], v29, s[48:51], s63 offen offset:3072 sc1\n"
    "v_mfma_f32_32x32x16_bf16 v[124:139], a[12:15], v[44:47], v[124:139]\n"
    "ds_read_b128 v[36:39], v10 offset:2048\n"
    "v_mul_f32 v144, v80, v80\n"
    "v_fma_f32 v144, v144, v30, v31\n"
    "v_mul_f32 v144, v80, v144\n"
    "v_exp_f32 v144, v144\n"
    "v_mul_f32 v146, v80, v88\n"
    "v_add_f32 v144, 1.0, v144\n"
    "s_waitcnt lgkmcnt(2)\n"
    "v_mfma_f32_32x32x16_bf16 v[124:139], a[16:19], v[48:51], v[124:139]\n"
    "ds_read_b128 v[40:43], v11 offset:2048\n"
    "s_add_u32 s63, s81, 0xb000\n"
    "buffer_load_dwordx4 v[240:243], v29, s[48:51], s63 offen offset:0 sc1\n"
    "v_rcp_f32 v144, v144\n"
    "v_mfma_f32_32x32x16_bf16 v[124:139], a[20:23], v[52:55], v[124:139]\n"
    "ds_read_b128 v[44:47], v12 offset:2048\n"
    "v_mul_f32 v145, v81, v81\n"
    "v_mul_f32 v146, v146, v144\n"
    "v_fma_f32 v145, v145, v30, v31\n"
    "v_mul_f32 v145, v81, v145\n"
    "v_exp_f32 v145, v145\n"
    "v_mul_f32 v147, v81, v89\n"
    "v_mfma_f32_32x32x16_bf16 v[124:139], a[24:27], v[56:59], v[124:139]\n"
    "ds_read_b128 v[48:51], v9 offset:3072\n"
    "buffer_load_dwordx4 v[244:247], v29, s[48:51], s63 offen offset:1024 sc1\n"
    "v_add_f32 v145, 1.0, v145\n"
    "v_rcp_f32 v145, v145\n"
    "v_mul_f32 v144, v82, v82\n"
    "v_mfma_f32_32x32x16_bf16 v[124:139], a[28:31], v[60:63], v[124:139]\n"
    "ds_read_b128 v[52:55], v10 offset:3072\n"
    "v_mul_f32 v147, v147, v145\n"
    "v_cvt_pk_bf16_f32 v148, v146, v147\n"
    "v_fma_f32 v144, v144, v30, v31\n"
    "v_mul_f32 v144, v82, v144\n"
    "v_exp_f32 v144, v144\n"
    "v_mul_f32 v146, v82, v90\n"
    "s_waitcnt lgkmcnt(2)\n"
    "v_mfma_f32_32x32x16_bf16 v[124:139], a[32:35], v[32:35], v[124:139]\n"
    "ds_read_b128 v[56:59], v11 offset:3072\n"
    "buffer_load_dwordx4 v[248:251], v29, s[48:51], s63 offen offset:2048 sc1\n"
    "v_add_f32 v144, 1.0, v144\n"
    "v_rcp_f32 v144, v144\n"
    "v_mfma_f32_32x32x16_bf16 v[124:139], a[36:39], v[36:39], v[124:139]\n"
    "ds_read_b128 v[60:63], v12 offset:3072\n"
    "v_mul_f32 v145, v83, v83\n"
    "v_mul_f32 v146, v146, v144\n"
    "v_fma_f32 v145, v145, v30, v31\n"
    "v_mul_f32 v145, v83, v145\n"
    "v_exp_f32 v145, v145\n"
    "v_mul_f32 v147, v83, v91\n"
    "v_mfma_f32_32x32x16_bf16 v[124:139], a[40:43], v[40:43], v[124:139]\n"
    "ds_read_b128 v[32:35], v9 offset:16384\n"
    "buffer_load_dwordx4 v[252:255], v29, s[48:51], s63 offen offset:3072 sc1\n"
    "v_add_f32 v145, 1.0, v145\n"
    "v_rcp_f32 v145, v145\n"
    "v_mul_f32 v144, v84, v84\n"
    "v_mfma_f32_32x32x16_bf16 v[124:139], a[44:47], v[44:47], v[124:139]\n"
    "ds_read_b128 v[36:39], v10 offset:16384\n"
    "v_mul_f32 v147, v147, v145\n"
    "v_cvt_pk_bf16_f32 v149, v146, v147\n"
    "v_fma_f32 v144, v144, v30, v31\n"
    "v_mul_f32 v144, v84, v144\n"
    "v_exp_f32 v144, v144\n"
    "v_mul_f32 v146, v84, v92\n"
    "s_waitcnt lgkmcnt(2)\n"
    "v_mfma_f32_32x32x16_bf16 v[124:139], a[48:51], v[48:51], v[124:139]\n"
    "ds_read_b128 v[40:43], v11 offset:16384\n"
    "v_add_f32 v144, 1.0, v144\n"
    "v_rcp_f32 v144, v144\n"
    "v_mul_f32 v145, v85, v85\n"
    "v_mul_f32 v146, v146, v144\n"
    "v_fma_f32 v145, v145, v30, v31\n"
    "v_mfma_f32_32x32x16_bf16 v[124:139], a[52:55], v[52:55], v[124:139]\n"
    "ds_read_b128 v[44:47], v12 offset:16384\n"
    "v_mul_f32 v145, v85, v145\n"
    "v_exp_f32 v145, v145\n"
    "v_mul_f32 v147, v85, v93\n"
    "v_add_f32 v145, 1.0, v145\n"
    "v_mfma_f32_32x32x16_bf16 v[124:139], a[56:59], v[56:59], v[124:139]\n"
    "ds_read_b128 v[48:51], v9 offset:17408\n"
    "s_waitcnt vmcnt(9)\n"
    "v_cmp_eq_u64 vcc, s[56:57], v[156:157]\n"
    "s_and_b64 s[70:71], vcc, s[68:69]\n"
    "s_cmp_eq_u64 s[70:71], s[68:69]\n"
    "s_cbranch_scc1 L_ok2_%=\n"
    "s_mov_b32 s74, 0x4000\n"
    "L_spin2_%=:\n"
    "s_sleep 2\n"
    "s_add_u32 s82, s76, 0\n"
    "buffer_load_dwordx2 v[156:157], v159, s[48:51], s82 offen sc1\n"
    "s_waitcnt vmcnt(0)\n"
    "v_cmp_eq_u64 vcc, s[56:57], v[156:157]\n"
    "s_and_b64 s[70:71], vcc, s[68:69]\n"
    "s_cmp_eq_u64 s[70:71], s[68:69]\n"
    "s_cbranch_scc1 L_ok2_%=\n"
    "s_sub_u32 s74, s74, 1\n"
    "s_cmp_lg_u32 s74, 0\n"
    "s_cbranch_scc1 L_spin2_%=\n"
    "L_ok2_%=:\n"
    "v_rcp_f32 v145, v145\n"
    "v_mul_f32 v144, v86, v86\n"
    "v_mfma_f32_32x32x16_bf16 v[124:139], a[60:63], v[60:63], v[124:139]\n"
    "ds_read_b128 v[52:55], v10 offset:17408\n"
    "s_add_u32 s65, s80, 0x0\n"
    "buffer_load_dwordx4 a[0:3], v29, s[48:51], s65 offen offset:0 sc1\n"
    "v_mul_f32 v147, v147, v145\n"
    "v_cvt_pk_bf16_f32 v150, v146, v147\n"
    "v_fma_f32 v144, v144, v30, v31\n"
    "s_waitcnt lgkmcnt(2)\n"
    "v_mfma_f32_32x32x16_bf16 v[124:139], a[64:67], v[32:35], v[124:139]\n"
    "ds_read_b128 v[56:59], v11 offset:17408\n"
    "v_mul_f32 v144, v86, v144\n"
    "v_exp_f32 v144, v144\n"
    "v_mul_f32 v146, v86, v94\n"
    "v_add_f32 v144, 1.0, v144\n"
    "v_mfma_f32_32x32x16_bf16 v[124:139], a[68:71], v[36:39], v[124:139]\n"
    "ds_read_b128 v[60:63], v12 offset:17408\n"
    "buffer_load_dwordx4 a[4:7], v29, s[48:51], s65 offen offset:1024 sc1\n"
    "v_rcp_f32 v144, v144\n"
    "v_mul_f32 v145, v87, v87\n"
    "v_mul_f32 v146, v146, v144\n"
    "v_mfma_f32_32x32x16_bf16 v[124:139], a[72:75], v[40:43], v[124:139]\n"
    "ds_read_b128 v[32:35], v9 offset:18432\n"
    "v_fma_f32 v145, v145, v30, v31\n"
    "v_mul_f32 v145, v87, v145\n"
    "v_exp_f32 v145, v145\n"
    "v_mul_f32 v147, v87, v95\n"
    "v_add_f32 v145, 1.0, v145\n"
    "v_mfma_f32_32x32x16_bf16 v[124:139], a[76:79], v[44:47], v[124:139]\n"
    "ds_read_b128 v[36:39], v10 offset:18432\n"
    "buffer_load_dwordx4 a[8:11], v29, s[48:51], s65 offen offset:2048 sc1\n"
    "v_rcp_f32 v145, v145\n"
    "s_add_u32 s72, s61, 0xa2000\n"
    "v_mul_f32 v147, v147, v145\n"
    "s_waitcnt lgkmcnt(2)\n"
    "v_mfma_f32_32x32x16_bf16 v[124:139], a[80:83], v[48:51], v[124:139]\n"
    "ds_read_b128 v[40:43], v11 offset:18432\n"
    "v_cvt_pk_bf16_f32 v151, v146, v147\n"
    "buffer_store_dwordx4 v[148:151], v28, s[48:51], s72 offen offset:0 sc1\n"
    "v_mfma_f32_32x32x16_bf16 v[124:139], a[84:87], v[52:55], v[124:139]\n"
    "ds_read_b128 v[44:47], v12 offset:18432\n"
    "buffer_load_dwordx4 a[12:15], v29, s[48:51], s65 offen offset:3072 sc1\n"
    "v_mfma_f32_32x32x16_bf16 v[124:139], a[88:91], v[56:59], v[124:139]\n"
    "ds_read_b128 v[48:51], v9 offset:19456\n"
    "v_mfma_f32_32x32x16_bf16 v[124:139], a[92:95], v[60:63], v[124:139]\n"
    "ds_read_b128 v[52:55], v10 offset:19456\n"
    "s_add_u32 s65, s80, 0x1000\n"
    "buffer_load_dwordx4 a[16:19], v29, s[48:51], s65 offen offset:0 sc1\n"
    "s_waitcnt lgkmcnt(2)\n"
    "v_mfma_f32_32x32x16_bf16 v[124:139], a[96:99], v[32:35], v[124:139]\n"
    "ds_read_b128 v[56:59], v11 offset:19456\n"
    "v_mfma_f32_32x32x16_bf16 v[124:139], a[100:103], v[36:39], v[124:139]\n"
    "ds_read_b128 v[60:63], v12 offset:19456\n"
    "buffer_load_dwordx4 a[20:23], v29, s[48:51], s65 offen offset:1024 sc1\n"
    "v_mfma_f32_32x32x16_bf16 v[124:139], a[104:107], v[40:43], v[124:139]\n"
    "ds_read_b128 v[32:35], v5 offset:51200\n"
    "s_waitcnt vmcnt(13)\n"
    "s_barrier\n"
    "s_add_u32 s82, s75, 65536\n"
    "buffer_store_dwordx2 v[24:25], v158, s[48:51], s82 offen offset:0 sc1\n"
    "v_mfma_f32_32x32x16_bf16 v[124:139], a[108:111], v[44:47], v[124:139]\n"
    "ds_read_b128 v[36:39], v6 offset:51200\n"
    "buffer_load_dwordx4 a[24:27], v29, s[48:51], s65 offen offset:2048 sc1\n"
    "s_waitcnt lgkmcnt(2)\n"
    "v_mfma_f32_32x32x16_bf16 v[124:139], a[112:115], v[48:51], v[124:139]\n"
    "ds_read_b128 v[40:43], v7 offset:51200\n"
    "v_mfma_f32_32x32x16_bf16 v[124:139], a[116:119], v[52:55], v[124:139]\n"
    "ds_read_b128 v[44:47], v8 offset:51200\n"
    "buffer_load_dwordx4 a[28:31], v29, s[48:51], s65 offen offset:3072 sc1\n"
    "v_mfma_f32_32x32x16_bf16 v[124:139], a[120:123], v[56:59], v[124:139]\n"
    "ds_read_b128 v[48:51], v5 offset:52224\n"
    "v_mfma_f32_32x32x16_bf16 v[124:139], a[124:127], v[60:63], v[124:139]\n"
    "ds_read_b128 v[52:55], v6 offset:52224\n"
    "s_add_u32 s65, s80, 0x2000\n"
    "buffer_load_dwordx4 a[32:35], v29, s[48:51], s65 offen offset:0 sc1\n"
    "s_waitcnt lgkmcnt(2)\n"
    "v_mfma_f32_32x32x16_bf16 v[124:139], a[128:131], v[32:35], v[124:139]\n"
    "ds_read_b128 v[56:59], v7 offset:52224\n"
    "v_mfma_f32_32x32x16_bf16 v[124:139], a[132:135], v[36:39], v[124:139]\n"
    "ds_read_b128 v[60:63], v8 offset:52224\n"
    "buffer_load_dwordx4 a[36:39], v29, s[48:51], s65 offen offset:1024 sc1\n"
    "v_mfma_f32_32x32x16_bf16 v[124:139], a[136:139], v[40:43], v[124:139]\n"
    "v_mfma_f32_32x32x16_bf16 v[124:139], a[140:143], v[44:47], v[124:139]\n"
    "buffer_load_dwordx4 a[40:43], v29, s[48:51], s65 offen offset:2048 sc1\n"
    "s_waitcnt lgkmcnt(0)\n"
    "v_mfma_f32_32x32x16_bf16 v[124:139], a[144:147], v[48:51], v[124:139]\n"
    "v_mfma_f32_32x32x16_bf16 v[124:139], a[148:151], v[52:55], v[124:139]\n"
    "buffer_load_dwordx4 a[44:47], v29, s[48:51], s65 offen offset:3072 sc1\n"
    "v_mfma_f32_32x32x16_bf16 v[124:139], a[152:155], v[56:59], v[124:139]\n"
    "v_mfma_f32_32x32x16_bf16 v[124:139], a[156:159], v[60:63], v[124:139]\n"
    "s_add_u32 s65, s78, 0x3000\n"
    "buffer_load_dwordx4 a[48:51], v29, s[48:51], s65 offen offset:0 sc1\n"
    "buffer_load_dwordx4 a[52:55], v29, s[48:51], s65 offen offset:1024 sc1\n"
    "s_add_u32 s82, s76, 32768\n"
    "buffer_load_dwordx2 v[156:157], v159, s[48:51], s82 offen offset:0 sc1\n"
    "s_branch L_join_%=\n"
    "L_helper_%=:\n"
    "s_mov_b32 s56, %[tag_lo]\n"
    "s_mov_b32 s57, %[tag_hi]\n"
    "s_mov_b32 s75, 0\n"
    "v_lshlrev_b32 v29, 4, v0\n"
    "v_lshl_add_u32 v21, s77, 12, v29\n"
    "v_add_u32 v22, 0x10000, v21\n"
    "s_cmp_eq_u32 s77, 3\n"
    "s_cselect_b32 s70, 6, 7\n"
    "s_bfm_b64 s[68:69], s70, 0\n"
    "s_mul_i32 s71, s77, 896\n"
    "v_lshl_add_u32 v159, v0, 7, s71\n"
    "v_mov_b32 v156, 0x40000000\n"
    "v_cmp_gt_u32 vcc, s70, v0\n"
    "s_nop 1\n"
    "v_cndmask_b32 v159, v156, v159, vcc\n"
    "v_or_b32 v158, s77, v0\n"
    "v_cmp_eq_u32 vcc, 0, v158\n"
    "s_nop 1\n"
    "v_cndmask_b32 v158, v156, v158, vcc\n"
    "v_mov_b32 v23, v159\n"
    "s_mov_b64 s[98:99], s[68:69]\n"
    "s_mul_i32 s81, s74, 276480\n"
    "s_mul_i32 s70, s77, 71680\n"
    "s_add_u32 s81, s81, s70\n"
    "s_add_u32 s81, s81, 7340032\n"
    "s_cmp_eq_u32 s77, 3\n"
    "s_cselect_b32 s64, 0x40000000, s81\n"
    "s_lshl_b32 s70, s67, 4\n"
    "s_add_u32 s70, s70, s83\n"
    "s_mul_i32 s80, s70, 55296\n"
    "s_mul_i32 s71, s77, 14336\n"
    "s_add_u32 s80, s80, s71\n"
    "s_add_u32 s80, s80, 262144\n"
    "s_cmp_eq_u32 s77, 3\n"
    "s_cselect_b32 s78, 0x40000000, s80\n"
    "s_lshl_b32 s70, s70, 4\n"
    "s_lshl_b32 s71, s77, 2\n"
    "s_add_u32 s70, s70, s71\n"
    "s_mul_i32 s66, s70, 1280\n"
    "s_mul_i32 s70, s74, 160\n"
    "s_add_u32 s66, s66, s70\n"
    "s_lshl_b32 s76, s67, 12\n"
    "s_add_u32 s76, s76, 65536\n"
    "v_mov_b32 v158, 0x40000000\n"
    "v_mov_b32 v28, 0x40000000\n"
    "s_mov_b32 s61, 0\n"
    "v_mov_b32 v30, 0\n"
    "v_mov_b32 v31, 0\n"
    "v_mul_u32_u24 v144, 0x199a, v0\n"
    "v_lshrrev_b32 v144, 16, v144\n"
    "v_mul_u32_u24 v145, 10, v144\n"
    "v_sub_u32 v145, v0, v145\n"
    "v_mul_u32_u24 v18, 0x500, v144\n"
    "v_lshl_add_u32 v18, v145, 4, v18\n"
    "v_mov_b32 v146, 0x40000000\n"
    "v_cmp_gt_u32 vcc, 40, v0\n"
    "s_nop 1\n"
    "v_cndmask_b32 v18, v146, v18, vcc\n"
    "s_lshl_b32 s70, s77, 2\n"
    "v_add_u32 v144, s70, v144\n"
    "v_min_u32 v144, 15, v144\n"
    "v_and_b32 v147, 3, v145\n"
    "v_lshl_add_u32 v144, v147, 2, v144\n"
    "v_and_b32 v144, 15, v144\n"
    "v_lshrrev_b32 v146, 2, v145\n"
    "v_lshlrev_b32 v146, 11, v146\n"
    "v_lshl_add_u32 v146, v147, 8, v146\n"
    "v_lshl_add_u32 v16, v144, 4, v146\n"
    "v_add_u32 v17, 0x400, v16\n"
    "v_subrev_u32 v147, 8, v145\n"
    "v_lshlrev_b32 v147, 9, v147\n"
    "v_lshl_add_u32 v147, v144, 4, v147\n"
    "v_add_u32 v147, 0x1000, v147\n"
    "v_add_u32 v146, 0x100, v147\n"
    "v_cmp_gt_u32 vcc, 8, v145\n"
    "s_nop 1\n"
    "v_cndmask_b32 v16, v147, v16, vcc\n"
    "v_cndmask_b32 v17, v146, v17, vcc\n"
    "s_mul_i32 s70, s77, 5120\n"
    "v_lshrrev_b32 v145, 4, v0\n"
    "v_lshl_add_u32 v146, v145, 8, s70\n"
    "v_lshl_add_u32 v147, v145, 2, v0\n"
    "v_and_b32 v147, 15, v147\n"
    "v_lshl_add_u32 v19, v147, 4, v146\n"
    "v_lshrrev_b32 v145, 1, v145\n"
    "v_lshl_add_u32 v147, v145, 2, v0\n"
    "v_and_b32 v147, 15, v147\n"
    "v_lshl_add_u32 v20, v147, 4, v146\n"
    "v_add_u32 v16, 0x1e000, v16\n"
    "v_add_u32 v17, 0x1e000, v17\n"
    "v_add_u32 v19, 0x1e000, v19\n"
    "v_add_u32 v20, 0x1e000, v20\n"
    "s_mov_b32 s82, 4096\n"
    "buffer_load_dwordx2 v[156:157], v23, s[48:51], s82 offen offset:0 sc1\n"
    "s_waitcnt vmcnt(0)\n"
    "v_cmp_eq_u64 vcc, s[56:57], v[156:157]\n"
    "s_and_b64 s[70:71], vcc, s[98:99]\n"
    "s_cmp_eq_u64 s[70:71], s[98:99]\n"
    "s_cbranch_scc1 L_ok3_%=\n"
    "s_mov_b32 s74, 0x4000\n"
    "L_spin3_%=:\n"
    "s_sleep 2\n"
    "s_mov_b32 s82, 4096\n"
    "buffer_load_dwordx2 v[156:157], v23, s[48:51], s82 offen sc1\n"
    "s_waitcnt vmcnt(0)\n"
    "v_cmp_eq_u64 vcc, s[56:57], v[156:157]\n"
    "s_and_b64 s[70:71], vcc, s[98:99]\n"
    "s_cmp_eq_u64 s[70:71], s[98:99]\n"
    "s_cbranch_scc1 L_ok3_%=\n"
    "s_sub_u32 s74, s74, 1\n"
    "s_cmp_lg_u32 s74, 0\n"
    "s_cbranch_scc1 L_spin3_%=\n"
    "L_ok3_%=:\n"
    "s_add_u32 s63, s81, 0x0\n"
    "buffer_load_dwordx4 a[160:163], v29, s[48:51], s63 offen offset:0 sc1\n"
    "buffer_load_dwordx4 a[164:167], v29, s[48:51], s63 offen offset:1024 sc1\n"
    "buffer_load_dwordx4 a[168:171], v29, s[48:51], s63 offen offset:2048 sc1\n"
    "buffer_load_dwordx4 a[172:175], v29, s[48:51], s63 offen offset:3072 sc1\n"
    "s_add_u32 s63, s81, 0x1000\n"
    "buffer_load_dwordx4 a[176:179], v29, s[48:51], s63 offen offset:0 sc1\n"
    "buffer_load_dwordx4 a[180:183], v29, s[48:51], s63 offen offset:1024 sc1\n"
    "buffer_load_dwordx4 a[184:187], v29, s[48:51], s63 offen offset:2048 sc1\n"
    "buffer_load_dwordx4 a[188:191], v29, s[48:51], s63 offen offset:3072 sc1\n"
    "s_add_u32 s63, s81, 0x2000\n"
    "buffer_load_dwordx4 a[192:195], v29, s[48:51], s63 offen offset:0 sc1\n"
    "buffer_load_dwordx4 a[196:199], v29, s[48:51], s63 offen offset:1024 sc1\n"
    "buffer_load_dwordx4 a[200:203], v29, s[48:51], s63 offen offset:2048 sc1\n"
    "buffer_load_dwordx4 a[204:207], v29, s[48:51], s63 offen offset:3072 sc1\n"
    "s_add_u32 s63, s81, 0x3000\n"
    "buffer_load_dwordx4 a[208:211], v29, s[48:51], s63 offen offset:0 sc1\n"
    "buffer_load_dwordx4 a[212:215], v29, s[48:51], s63 offen offset:1024 sc1\n"
    "buffer_load_dwordx4 a[216:219], v29, s[48:51], s63 offen offset:2048 sc1\n"
    "buffer_load_dwordx4 a[220:223], v29, s[48:51], s63 offen offset:3072 sc1\n"
    "s_add_u32 s63, s81, 0x4000\n"
    "buffer_load_dwordx4 a[224:227], v29, s[48:51], s63 offen offset:0 sc1\n"
    "buffer_load_dwordx4 a[228:231], v29, s[48:51], s63 offen offset:1024 sc1\n"
    "buffer_load_dwordx4 a[232:235], v29, s[48:51], s63 offen offset:2048 sc1\n"
    "buffer_load_dwordx4 a[236:239], v29, s[48:51], s63 offen offset:3072 sc1\n"
    "s_add_u32 s63, s81, 0x5000\n"
    "buffer_load_dwordx4 a[240:243], v29, s[48:51], s63 offen offset:0 sc1\n"
    "buffer_load_dwordx4 a[244:247], v29, s[48:51], s63 offen offset:1024 sc1\n"
    "buffer_load_dwordx4 a[248:251], v29, s[48:51], s63 offen offset:2048 sc1\n"
    "buffer_load_dwordx4 a[252:255], v29, s[48:51], s63 offen offset:3072 sc1\n"
    "s_add_u32 s63, s81, 0x6000\n"
    "buffer_load_dwordx4 v[160:163], v29, s[48:51], s63 offen offset:0 sc1\n"
    "buffer_load_dwordx4 v[164:167], v29, s[48:51], s63 offen offset:1024 sc1\n"
    "buffer_load_dwordx4 v[168:171], v29, s[48:51], s63 offen offset:2048 sc1\n"
    "buffer_load_dwordx4 v[172:175], v29, s[48:51], s63 offen offset:3072 sc1\n"
    "s_add_u32 s63, s81, 0x7000\n"
    "buffer_load_dwordx4 v[176:179], v29, s[48:51], s63 offen offset:0 sc1\n"
    "buffer_load_dwordx4 v[180:183], v29, s[48:51], s63 offen offset:1024 sc1\n"
    "buffer_load_dwordx4 v[184:187], v29, s[48:51], s63 offen offset:2048 sc1\n"
    "buffer_load_dwordx4 v[188:191], v29, s[48:51], s63 offen offset:3072 sc1\n"
    "s_add_u32 s63, s81, 0x8000\n"
    "buffer_load_dwordx4 v[192:195], v29, s[48:51], s63 offen offset:0 sc1\n"
    "buffer_load_dwordx4 v[196:199], v29, s[48:51], s63 offen offset:1024 sc1\n"
    "buffer_load_dwordx4 v[200:203], v29, s[48:51], s63 offen offset:2048 sc1\n"
    "buffer_load_dwordx4 v[204:207], v29, s[48:51], s63 offen offset:3072 sc1\n"
    "s_add_u32 s63, s81, 0x9000\n"
    "buffer_load_dwordx4 v[208:211], v29, s[48:51], s63 offen offset:0 sc1\n"
    "buffer_load_dwordx4 v[212:215], v29, s[48:51], s63 offen offset:1024 sc1\n"
    "buffer_load_dwordx4 v[216:219], v29, s[48:51], s63 offen offset:2048 sc1\n"
    "buffer_load_dwordx4 v[220:223], v29, s[48:51], s63 offen offset:3072 sc1\n"
    "s_add_u32 s63, s81, 0xa000\n"
    "buffer_load_dwordx4 v[224:227], v29, s[48:51], s63 offen offset:0 sc1\n"
    "buffer_load_dwordx4 v[228:231], v29, s[48:51], s63 offen offset:1024 sc1\n"
    "buffer_load_dwordx4 v[232:235], v29, s[48:51], s63 offen offset:2048 sc1\n"
    "buffer_load_dwordx4 v[236:239], v29, s[48:51], s63 offen offset:3072 sc1\n"
    "s_add_u32 s63, s81, 0xb000\n"
    "buffer_load_dwordx4 v[240:243], v29, s[48:51], s63 offen offset:0 sc1\n"
    "buffer_load_dwordx4 v[244:247], v29, s[48:51], s63 offen offset:1024 sc1\n"
    "buffer_load_dwordx4 v[248:251], v29, s[48:51], s63 offen offset:2048 sc1\n"
    "buffer_load_dwordx4 v[252:255], v29, s[48:51], s63 offen offset:3072 sc1\n"
    "s_add_u32 s63, s81, 0xc000\n"
    "buffer_load_dwordx4 a[112:115], v29, s[48:51], s63 offen offset:0 sc1\n"
    "buffer_load_dwordx4 a[116:119], v29, s[48:51], s63 offen offset:1024 sc1\n"
    "buffer_load_dwordx4 a[120:123], v29, s[48:51], s63 offen offset:2048 sc1\n"
    "buffer_load_dwordx4 a[124:127], v29, s[48:51], s63 offen offset:3072 sc1\n"
    "s_add_u32 s63, s81, 0xd000\n"
    "buffer_load_dwordx4 a[128:131], v29, s[48:51], s63 offen offset:0 sc1\n"
    "buffer_load_dwordx4 a[132:135], v29, s[48:51], s63 offen offset:1024 sc1\n"
    "buffer_load_dwordx4 a[136:139], v29, s[48:51], s63 offen offset:2048 sc1\n"
    "buffer_load_dwordx4 a[140:143], v29, s[48:51], s63 offen offset:3072 sc1\n"
    "s_add_u32 s63, s81, 0xe000\n"
    "buffer_load_dwordx4 a[144:147], v29, s[48:51], s63 offen offset:0 sc1\n"
    "buffer_load_dwordx4 a[148:151], v29, s[48:51], s63 offen offset:1024 sc1\n"
    "buffer_load_dwordx4 a[152:155], v29, s[48:51], s63 offen offset:2048 sc1\n"
    "buffer_load_dwordx4 a[156:159], v29, s[48:51], s63 offen offset:3072 sc1\n"
    "s_add_u32 s63, s64, 0xf000\n"
    "buffer_load_dwordx4 v[64:67], v29, s[48:51], s63 offen offset:0 sc1\n"
    "buffer_load_dwordx4 v[68:71], v29, s[48:51], s63 offen offset:1024 sc1\n"
    "buffer_load_dwordx4 v[72:75], v29, s[48:51], s63 offen offset:2048 sc1\n"
    "buffer_load_dwordx4 v[76:79], v29, s[48:51], s63 offen offset:3072 sc1\n"
    "s_add_u32 s63, s64, 0x10000\n"
    "buffer_load_dwordx4 v[80:83], v29, s[48:51], s63 offen offset:0 sc1\n"
    "buffer_load_dwordx4 v[84:87], v29, s[48:51], s63 offen offset:1024 sc1\n"
    "buffer_load_dwordx4 v[88:91], v29, s[48:51], s63 offen offset:2048 sc1\n"
    "buffer_load_dwordx4 v[92:95], v29, s[48:51], s63 offen offset:3072 sc1\n"
    "s_add_u32 s63, s64, 0x11000\n"
    "buffer_load_dwordx4 v[96:99], v29, s[48:51], s63 offen offset:0 sc1\n"
    "buffer_load_dwordx4 v[100:103], v29, s[48:51], s63 offen offset:1024 sc1\n"
    "s_add_u32 s82, s76, 0\n"
    "buffer_load_dwordx2 v[156:157], v159, s[48:51], s82 offen offset:0 sc1\n"
    "s_waitcnt vmcnt(0)\n"
    "v_cmp_eq_u64 vcc, s[56:57], v[156:157]\n"
    "s_and_b64 s[70:71], vcc, s[68:69]\n"
    "s_cmp_eq_u64 s[70:71], s[68:69]\n"
    "s_cbranch_scc1 L_ok4_%=\n"
    "s_mov_b32 s74, 0x4000\n"
    "L_spin4_%=:\n"
    "s_sleep 2\n"
    "s_add_u32 s82, s76, 0\n"
    "buffer_load_dwordx2 v[156:157], v159, s[48:51], s82 offen sc1\n"
    "s_waitcnt vmcnt(0)\n"
    "v_cmp_eq_u64 vcc, s[56:57], v[156:157]\n"
    "s_and_b64 s[70:71], vcc, s[68:69]\n"
    "s_cmp_eq_u64 s[70:71], s[68:69]\n"
    "s_cbranch_scc1 L_ok4_%=\n"
    "s_sub_u32 s74, s74, 1\n"
    "s_cmp_lg_u32 s74, 0\n"
    "s_cbranch_scc1 L_spin4_%=\n"
    "L_ok4_%=:\n"
    "s_add_u32 s65, s80, 0x0\n"
    "buffer_load_dwordx4 a[0:3], v29, s[48:51], s65 offen offset:0 sc1\n"
    "buffer_load_dwordx4 a[4:7], v29, s[48:51], s65 offen offset:1024 sc1\n"
    "buffer_load_dwordx4 a[8:11], v29, s[48:51], s65 offen offset:2048 sc1\n"
    "buffer_load_dwordx4 a[12:15], v29, s[48:51], s65 offen offset:3072 sc1\n"
    "s_add_u32 s65, s80, 0x1000\n"
    "buffer_load_dwordx4 a[16:19], v29, s[48:51], s65 offen offset:0 sc1\n"
    "buffer_load_dwordx4 a[20:23], v29, s[48:51], s65 offen offset:1024 sc1\n"
    "buffer_load_dwordx4 a[24:27], v29, s[48:51], s65 offen offset:2048 sc1\n"
    "buffer_load_dwordx4 a[28:31], v29, s[48:51], s65 offen offset:3072 sc1\n"
    "s_add_u32 s65, s80, 0x2000\n"
    "buffer_load_dwordx4 a[32:35], v29, s[48:51], s65 offen offset:0 sc1\n"
    "buffer_load_dwordx4 a[36:39], v29, s[48:51], s65 offen offset:1024 sc1\n"
    "buffer_load_dwordx4 a[40:43], v29, s[48:51], s65 offen offset:2048 sc1\n"
    "buffer_load_dwordx4 a[44:47], v29, s[48:51], s65 offen offset:3072 sc1\n"
    "s_add_u32 s65, s78, 0x3000\n"
    "buffer_load_dwordx4 a[48:51], v29, s[48:51], s65 offen offset:0 sc1\n"
    "buffer_load_dwordx4 a[52:55], v29, s[48:51], s65 offen offset:1024 sc1\n"
    "s_add_u32 s82, s76, 32768\n"
    "buffer_load_dwordx2 v[156:157], v159, s[48:51], s82 offen offset:0 sc1\n"
    "s_waitcnt vmcnt(0)\n"
    "L_join_%=:\n"
    "s_waitcnt vmcnt(16)\n"
    "v_mfma_f32_16x16x32_bf16 v[104:107], a[160:163], a[0:3], 0\n"
    "s_add_u32 s63, s81, 0xc000\n"
    "buffer_load_dwordx4 a[112:115], v29, s[48:51], s63 offen offset:0 sc1\n"
    "v_mfma_f32_16x16x32_bf16 v[108:111], a[164:167], a[0:3], 0\n"
    "buffer_load_dwordx4 a[116:119], v29, s[48:51], s63 offen offset:1024 sc1\n"
    "v_mfma_f32_16x16x32_bf16 v[112:115], a[168:171], a[0:3], 0\n"
    "v_mul_f32 v144, v124, v124\n"
    "v_fma_f32 v144, v144, v30, v31\n"
    "v_mul_f32 v144, v124, v144\n"
    "v_exp_f32 v144, v144\n"
    "v_mul_f32 v146, v124, v132\n"
    "v_add_f32 v144, 1.0, v144\n"
    "v_mfma_f32_16x16x32_bf16 v[116:119], a[172:175], a[0:3], 0\n"
    "v_rcp_f32 v144, v144\n"
    "v_mul_f32 v145, v125, v125\n"
    "v_mul_f32 v146, v146, v144\n"
    "v_fma_f32 v145, v145, v30, v31\n"
    "v_mul_f32 v145, v125, v145\n"
    "buffer_load_dwordx4 a[120:123], v29, s[48:51], s63 offen offset:2048 sc1\n"
    "v_mfma_f32_16x16x32_bf16 v[120:123], a[176:179], a[0:3], 0\n"
    "v_exp_f32 v145, v145\n"
    "v_mul_f32 v147, v125, v133\n"
    "v_add_f32 v145, 1.0, v145\n"
    "buffer_load_dwordx4 a[124:127], v29, s[48:51], s63 offen offset:3072 sc1\n"
    "s_waitcnt vmcnt(19)\n"
    "v_mfma_f32_16x16x32_bf16 v[104:107], a[180:183], a[4:7], v[104:107]\n"
    "v_rcp_f32 v145, v145\n"
    "v_mul_f32 v144, v126, v126\n"
    "v_mul_f32 v147, v147, v145\n"
    "v_cvt_pk_bf16_f32 v152, v146, v147\n"
    "v_fma_f32 v144, v144, v30, v31\n"
    "v_mul_f32 v144, v126, v144\n"
    "v_mfma_f32_16x16x32_bf16 v[108:111], a[184:187], a[4:7], v[108:111]\n"
    "v_exp_f32 v144, v144\n"
    "v_mul_f32 v146, v126, v134\n"
    "v_add_f32 v144, 1.0, v144\n"
    "s_add_u32 s63, s81, 0xd000\n"
    "buffer_load_dwordx4 a[128:131], v29, s[48:51], s63 offen offset:0 sc1\n"
    "v_mfma_f32_16x16x32_bf16 v[112:115], a[188:191], a[4:7], v[112:115]\n"
    "v_rcp_f32 v144, v144\n"
    "v_mul_f32 v145, v127, v127\n"
    "v_mul_f32 v146, v146, v144\n"
    "v_fma_f32 v145, v145, v30, v31\n"
    "v_mul_f32 v145, v127, v145\n"
    "buffer_load_dwordx4 a[132:135], v29, s[48:51], s63 offen offset:1024 sc1\n"
    "v_mfma_f32_16x16x32_bf16 v[116:119], a[192:195], a[4:7], v[116:119]\n"
    "v_exp_f32 v145, v145\n"
    "v_mul_f32 v147, v127, v135\n"
    "v_add_f32 v145, 1.0, v145\n"
    "v_mfma_f32_16x16x32_bf16 v[120:123], a[196:199], a[4:7], v[120:123]\n"
    "v_rcp_f32 v145, v145\n"
    "v_mul_f32 v144, v128, v128\n"
    "v_mul_f32 v147, v147, v145\n"
    "v_cvt_pk_bf16_f32 v153, v146, v147\n"
    "v_fma_f32 v144, v144, v30, v31\n"
    "v_mul_f32 v144, v128, v144\n"
    "s_waitcnt vmcnt(20)\n"
    "v_mfma_f32_16x16x32_bf16 v[104:107], a[200:203], a[8:11], v[104:107]\n"
    "v_exp_f32 v144, v144\n"
    "v_mul_f32 v146, v128, v136\n"
    "v_add_f32 v144, 1.0, v144\n"
    "buffer_load_dwordx4 a[136:139], v29, s[48:51], s63 offen offset:2048 sc1\n"
    "v_mfma_f32_16x16x32_bf16 v[108:111], a[204:207], a[8:11], v[108:111]\n"
    "v_rcp_f32 v144, v144\n"
    "v_mul_f32 v145, v129, v129\n"
    "v_mul_f32 v146, v146, v144\n"
    "v_fma_f32 v145, v145, v30, v31\n"
    "v_mul_f32 v145, v129, v145\n"
    "buffer_load_dwordx4 a[140:143], v29, s[48:51], s63 offen offset:3072 sc1\n"
    "v_mfma_f32_16x16x32_bf16 v[112:115], a[208:211], a[8:11], v[112:115]\n"
    "v_exp_f32 v145, v145\n"
    "v_mul_f32 v147, v129, v137\n"
    "v_add_f32 v145, 1.0, v145\n"
    "s_add_u32 s63, s81, 0xe000\n"
    "buffer_load_dwordx4 a[144:147], v29, s[48:51], s63 offen offset:0 sc1\n"
    "v_mfma_f32_16x16x32_bf16 v[116:119], a[212:215], a[8:11], v[116:119]\n"
    "v_rcp_f32 v145, v145\n"
    "v_mul_f32 v144, v130, v130\n"
    "v_mul_f32 v147, v147, v145\n"
    "v_cvt_pk_bf16_f32 v154, v146, v147\n"
    "v_fma_f32 v144, v144, v30, v31\n"
    "v_mul_f32 v144, v130, v144\n"
    "v_mfma_f32_16x16x32_bf16 v[120:123], a[216:219], a[8:11], v[120:123]\n"
    "v_exp_f32 v144, v144\n"
    "v_mul_f32 v146, v130, v138\n"
    "v_add_f32 v144, 1.0, v144\n"
    "buffer_load_dwordx4 a[148:151], v29, s[48:51], s63 offen offset:1024 sc1\n"
    "s_waitcnt vmcnt(22)\n"
    "v_mfma_f32_16x16x32_bf16 v[104:107], a[220:223], a[12:15], v[104:107]\n"
    "v_rcp_f32 v144, v144\n"
    "v_mul_f32 v145, v131, v131\n"
    "v_mul_f32 v146, v146, v144\n"
    "v_fma_f32 v145, v145, v30, v31\n"
    "v_mul_f32 v145, v131, v145\n"
    "v_mfma_f32_16x16x32_bf16 v[108:111], a[224:227], a[12:15], v[108:111]\n"
    "v_exp_f32 v145, v145\n"
    "v_mul_f32 v147, v131, v139\n"
    "v_add_f32 v145, 1.0, v145\n"
    "buffer_load_dwordx4 a[152:155], v29, s[48:51], s63 offen offset:2048 sc1\n"
    "v_mfma_f32_16x16x32_bf16 v[112:115], a[228:231], a[12:15], v[112:115]\n"
    "v_rcp_f32 v145, v145\n"
    "s_add_u32 s72, s61, 0xbd000\n"
    "v_mul_f32 v147, v147, v145\n"
    "v_cvt_pk_bf16_f32 v155, v146, v147\n"
    "buffer_store_dwordx4 v[152:155], v28, s[48:51], s72 offen offset:0 sc1\n"
    "v_mfma_f32_16x16x32_bf16 v[116:119], a[232:235], a[12:15], v[116:119]\n"
    "buffer_load_dwordx4 a[156:159], v29, s[48:51], s63 offen offset:3072 sc1\n"
    "s_add_u32 s63, s64, 0xf000\n"
    "v_mfma_f32_16x16x32_bf16 v[120:123], a[236:239], a[12:15], v[120:123]\n"
    "buffer_load_dwordx4 v[64:67], v29, s[48:51], s63 offen offset:0 sc1\n"
    "s_waitcnt vmcnt(25)\n"
    "v_mfma_f32_16x16x32_bf16 v[104:107], a[240:243], a[16:19], v[104:107]\n"
    "buffer_load_dwordx4 v[68:71], v29, s[48:51], s63 offen offset:1024 sc1\n"
    "v_mfma_f32_16x16x32_bf16 v[108:111], a[244:247], a[16:19], v[108:111]\n"
    "buffer_load_dwordx4 v[72:75], v29, s[48:51], s63 offen offset:2048 sc1\n"
    "v_mfma_f32_16x16x32_bf16 v[112:115], a[248:251], a[16:19], v[112:115]\n"
    "buffer_load_dwordx4 v[76:79], v29, s[48:51], s63 offen offset:3072 sc1\n"
    "v_mfma_f32_16x16x32_bf16 v[116:119], a[252:255], a[16:19], v[116:119]\n"
    "v_mfma_f32_16x16x32_bf16 v[120:123], v[160:163], a[16:19], v[120:123]\n"
    "s_add_u32 s63, s64, 0x10000\n"
    "buffer_load_dwordx4 v[80:83], v29, s[48:51], s63 offen offset:0 sc1\n"
    "s_waitcnt vmcnt(28)\n"
    "v_mfma_f32_16x16x32_bf16 v[104:107], v[164:167], a[20:23], v[104:107]\n"
    "buffer_load_dwordx4 v[84:87], v29, s[48:51], s63 offen offset:1024 sc1\n"
    "v_mfma_f32_16x16x32_bf16 v[108:111], v[168:171], a[20:23], v[108:111]\n"
    "v_mfma_f32_16x16x32_bf16 v[112:115], v[172:175], a[20:23], v[112:115]\n"
    "buffer_load_dwordx4 v[88:91], v29, s[48:51], s63 offen offset:2048 sc1\n"
    "v_mfma_f32_16x16x32_bf16 v[116:119], v[176:179], a[20:23], v[116:119]\n"
    "buffer_load_dwordx4 v[92:95], v29, s[48:51], s63 offen offset:3072 sc1\n"
    "v_mfma_f32_16x16x32_bf16 v[120:123], v[180:183], a[20:23], v[120:123]\n"
    "s_waitcnt vmcnt(29)\n"
    "v_mfma_f32_16x16x32_bf16 v[104:107], v[184:187], a[24:27], v[104:107]\n"
    "s_add_u32 s63, s64, 0x11000\n"
    "buffer_load_dwordx4 v[96:99], v29, s[48:51], s63 offen offset:0 sc1\n"
    "v_mfma_f32_16x16x32_bf16 v[108:111], v[188:191], a[24:27], v[108:111]\n"
    "buffer_load_dwordx4 v[100:103], v29, s[48:51], s63 offen offset:1024 sc1\n"
    "v_mfma_f32_16x16x32_bf16 v[112:115], v[192:195], a[24:27], v[112:115]\n"
    "v_mfma_f32_16x16x32_bf16 v[116:119], v[196:199], a[24:27], v[116:119]\n"
    "s_waitcnt vmcnt(23)\n"
    "v_cmp_eq_u64 vcc, s[56:57], v[156:157]\n"
    "s_and_b64 s[70:71], vcc, s[68:69]\n"
    "s_cmp_eq_u64 s[70:71], s[68:69]\n"
    "s_cbranch_scc1 L_ok5_%=\n"
    "s_mov_b32 s74, 0x4000\n"
    "L_spin5_%=:\n"
    "s_sleep 2\n"
    "s_add_u32 s82, s76, 32768\n"
    "buffer_load_dwordx2 v[156:157], v159, s[48:51], s82 offen sc1\n"
    "s_waitcnt vmcnt(0)\n"
    "v_cmp_eq_u64 vcc, s[56:57], v[156:157]\n"
    "s_and_b64 s[70:71], vcc, s[68:69]\n"
    "s_cmp_eq_u64 s[70:71], s[68:69]\n"
    "s_cbranch_scc1 L_ok5_%=\n"
    "s_sub_u32 s74, s74, 1\n"
    "s_cmp_lg_u32 s74, 0\n"
    "s_cbranch_scc1 L_spin5_%=\n"
    "L_ok5_%=:\n"
    "v_mfma_f32_16x16x32_bf16 v[120:123], v[200:203], a[24:27], v[120:123]\n"
    "s_add_u32 s65, s80, 0x36000\n"
    "buffer_load_dwordx4 a[56:59], v29, s[48:51], s65 offen offset:0 sc1\n"
    "v_mfma_f32_16x16x32_bf16 v[104:107], v[204:207], a[28:31], v[104:107]\n"
    "buffer_load_dwordx4 a[60:63], v29, s[48:51], s65 offen offset:1024 sc1\n"
    "v_mfma_f32_16x16x32_bf16 v[108:111], v[208:211], a[28:31], v[108:111]\n"
    "s_add_u32 s82, s76, 65536\n"
    "buffer_load_dwordx2 v[156:157], v159, s[48:51], s82 offen offset:0 sc1\n"
    "v_mfma_f32_16x16x32_bf16 v[112:115], v[212:215], a[28:31], v[112:115]\n"
    "buffer_load_dwordx4 a[64:67], v29, s[48:51], s65 offen offset:2048 sc1\n"
    "v_mfma_f32_16x16x32_bf16 v[116:119], v[216:219], a[28:31], v[116:119]\n"
    "v_mfma_f32_16x16x32_bf16 v[120:123], v[220:223], a[28:31], v[120:123]\n"
    "buffer_load_dwordx4 a[68:71], v29, s[48:51], s65 offen offset:3072 sc1\n"
    "v_mfma_f32_16x16x32_bf16 v[104:107], v[224:227], a[32:35], v[104:107]\n"
    "s_add_u32 s65, s80, 0x37000\n"
    "buffer_load_dwordx4 a[72:75], v29, s[48:51], s65 offen offset:0 sc1\n"
    "v_mfma_f32_16x16x32_bf16 v[108:111], v[228:231], a[32:35], v[108:111]\n"
    "v_mfma_f32_16x16x32_bf16 v[112:115], v[232:235], a[32:35], v[112:115]\n"
    "buffer_load_dwordx4 a[76:79], v29, s[48:51], s65 offen offset:1024 sc1\n"
    "v_mfma_f32_16x16x32_bf16 v[116:119], v[236:239], a[32:35], v[116:119]\n"
    "buffer_load_dwordx4 a[80:83], v29, s[48:51], s65 offen offset:2048 sc1\n"
    "v_mfma_f32_16x16x32_bf16 v[120:123], v[240:243], a[32:35], v[120:123]\n"
    "s_waitcnt vmcnt(29)\n"
    "v_mfma_f32_16x16x32_bf16 v[104:107], v[244:247], a[36:39], v[104:107]\n"
    "buffer_load_dwordx4 a[84:87], v29, s[48:51], s65 offen offset:3072 sc1\n"
    "v_mfma_f32_16x16x32_bf16 v[108:111], v[248:251], a[36:39], v[108:111]\n"
    "s_add_u32 s65, s80, 0x38000\n"
    "buffer_load_dwordx4 a[88:91], v29, s[48:51], s65 offen offset:0 sc1\n"
    "v_mfma_f32_16x16x32_bf16 v[112:115], v[252:255], a[36:39], v[112:115]\n"
    "v_mfma_f32_16x16x32_bf16 v[116:119], a[112:115], a[36:39], v[116:119]\n"
    "buffer_load_dwordx4 a[92:95], v29, s[48:51], s65 offen offset:1024 sc1\n"
    "v_mfma_f32_16x16x32_bf16 v[120:123], a[116:119], a[36:39], v[120:123]\n"
    "s_waitcnt vmcnt(22)\n"
    "s_barrier\n"
    "s_add_u32 s82, s75, 98304\n"
    "buffer_store_dwordx2 v[24:25], v158, s[48:51], s82 offen offset:0 sc1\n"
    "buffer_load_dwordx4 a[96:99], v29, s[48:51], s65 offen offset:2048 sc1\n"
    "v_mfma_f32_16x16x32_bf16 v[104:107], a[120:123], a[40:43], v[104:107]\n"
    "v_mfma_f32_16x16x32_bf16 v[108:111], a[124:127], a[40:43], v[108:111]\n"
    "buffer_load_dwordx4 a[100:103], v29, s[48:51], s65 offen offset:3072 sc1\n"
    "v_mfma_f32_16x16x32_bf16 v[112:115], a[128:131], a[40:43], v[112:115]\n"
    "s_add_u32 s65, s78, 0x39000\n"
    "buffer_load_dwordx4 a[104:107], v29, s[48:51], s65 offen offset:0 sc1\n"
    "v_mfma_f32_16x16x32_bf16 v[116:119], a[132:135], a[40:43], v[116:119]\n"
    "v_mfma_f32_16x16x32_bf16 v[120:123], a[136:139], a[40:43], v[120:123]\n"
    "buffer_load_dwordx4 a[108:111], v29, s[48:51], s65 offen offset:1024 sc1\n"
    "s_waitcnt vmcnt(26)\n"
    "v_mfma_f32_16x16x32_bf16 v[104:107], a[140:143], a[44:47], v[104:107]\n"
    "v_mfma_f32_16x16x32_bf16 v[108:111], a[144:147], a[44:47], v[108:111]\n"
    "v_mfma_f32_16x16x32_bf16 v[112:115], a[148:151], a[44:47], v[112:115]\n"
    "v_mfma_f32_16x16x32_bf16 v[116:119], a[152:155], a[44:47], v[116:119]\n"
    "v_mfma_f32_16x16x32_bf16 v[120:123], a[156:159], a[44:47], v[120:123]\n"
    "s_waitcnt vmcnt(21)\n"
    "v_mfma_f32_16x16x32_bf16 v[104:107], v[64:67], a[48:51], v[104:107]\n"
    "v_mfma_f32_16x16x32_bf16 v[108:111], v[68:71], a[48:51], v[108:111]\n"
    "v_mfma_f32_16x16x32_bf16 v[112:115], v[72:75], a[48:51], v[112:115]\n"
    "v_mfma_f32_16x16x32_bf16 v[116:119], v[76:79], a[48:51], v[116:119]\n"
    "v_mfma_f32_16x16x32_bf16 v[120:123], v[80:83], a[48:51], v[120:123]\n"
    "s_waitcnt vmcnt(16)\n"
    "v_mfma_f32_16x16x32_bf16 v[104:107], v[84:87], a[52:55], v[104:107]\n"
    "v_mfma_f32_16x16x32_bf16 v[108:111], v[88:91], a[52:55], v[108:111]\n"
    "v_mfma_f32_16x16x32_bf16 v[112:115], v[92:95], a[52:55], v[112:115]\n"
    "v_mfma_f32_16x16x32_bf16 v[116:119], v[96:99], a[52:55], v[116:119]\n"
    "v_mfma_f32_16x16x32_bf16 v[120:123], v[100:103], a[52:55], v[120:123]\n"
    "s_waitcnt vmcnt(13)\n"
    "v_cmp_eq_u64 vcc, s[56:57], v[156:157]\n"
    "s_and_b64 s[70:71], vcc, s[68:69]\n"
    "s_cmp_eq_u64 s[70:71], s[68:69]\n"
    "s_cbranch_scc1 L_ok6_%=\n"
    "s_mov_b32 s74, 0x4000\n"
    "L_spin6_%=:\n"
    "s_sleep 2\n"
    "s_add_u32 s82, s76, 65536\n"
    "buffer_load_dwordx2 v[156:157], v159, s[48:51], s82 offen sc1\n"
    "s_waitcnt vmcnt(0)\n"
    "v_cmp_eq_u64 vcc, s[56:57], v[156:157]\n"
    "s_and_b64 s[70:71], vcc, s[68:69]\n"
    "s_cmp_eq_u64 s[70:71], s[68:69]\n"
    "s_cbranch_scc1 L_ok6_%=\n"
    "s_sub_u32 s74, s74, 1\n"
    "s_cmp_lg_u32 s74, 0\n"
    "s_cbranch_scc1 L_spin6_%=\n"
    "L_ok6_%=:\n"
    "s_waitcnt vmcnt(8)\n"
    "v_mfma_f32_16x16x32_bf16 v[124:127], a[160:163], a[56:59], 0\n"
    "s_add_u32 s65, s80, 0x6c000\n"
    "v_mfma_f32_16x16x32_bf16 v[128:131], a[164:167], a[56:59], 0\n"
    "buffer_load_dwordx4 a[0:3], v29, s[48:51], s65 offen offset:0 sc1\n"
    "v_mfma_f32_16x16x32_bf16 v[132:135], a[168:171], a[56:59], 0\n"
    "v_mfma_f32_16x16x32_bf16 v[136:139], a[172:175], a[56:59], 0\n"
    "v_mfma_f32_16x16x32_bf16 v[140:143], a[176:179], a[56:59], 0\n"
    "buffer_load_dwordx4 a[4:7], v29, s[48:51], s65 offen offset:1024 sc1\n"
    "v_mfma_f32_16x16x32_bf16 v[124:127], a[180:183], a[60:63], v[124:127]\n"
    "v_mfma_f32_16x16x32_bf16 v[128:131], a[184:187], a[60:63], v[128:131]\n"
    "v_mfma_f32_16x16x32_bf16 v[132:135], a[188:191], a[60:63], v[132:135]\n"
    "v_mfma_f32_16x16x32_bf16 v[136:139], a[192:195], a[60:63], v[136:139]\n"
    "buffer_load_dwordx4 a[8:11], v29, s[48:51], s65 offen offset:2048 sc1\n"
    "v_mfma_f32_16x16x32_bf16 v[140:143], a[196:199], a[60:63], v[140:143]\n"
    "ds_write_b128 v19, v[104:107] offset:0\n"
    "v_mfma_f32_16x16x32_bf16 v[124:127], a[200:203], a[64:67], v[124:127]\n"
    "ds_write_b128 v19, v[108:111] offset:1024\n"
    "v_mfma_f32_16x16x32_bf16 v[128:131], a[204:207], a[64:67], v[128:131]\n"
    "ds_write_b128 v19, v[112:115] offset:2048\n"
    "v_mfma_f32_16x16x32_bf16 v[132:135], a[208:211], a[64:67], v[132:135]\n"
    "buffer_load_dwordx4 a[12:15], v29, s[48:51], s65 offen offset:3072 sc1\n"
    "v_mfma_f32_16x16x32_bf16 v[136:139], a[212:215], a[64:67], v[136:139]\n"
    "ds_write_b128 v19, v[116:119] offset:3072\n"
    "v_mfma_f32_16x16x32_bf16 v[140:143], a[216:219], a[64:67], v[140:143]\n"
    "ds_write_b128 v20, v[120:123] offset:4096\n"
    "v_mfma_f32_16x16x32_bf16 v[124:127], a[220:223], a[68:71], v[124:127]\n"
    "v_mfma_f32_16x16x32_bf16 v[128:131], a[224:227], a[68:71], v[128:131]\n"
    "s_add_u32 s65, s80, 0x6d000\n"
    "buffer_load_dwordx4 a[16:19], v29, s[48:51], s65 offen offset:0 sc1\n"
    "v_mfma_f32_16x16x32_bf16 v[132:135], a[228:231], a[68:71], v[132:135]\n"
    "v_mfma_f32_16x16x32_bf16 v[136:139], a[232:235], a[68:71], v[136:139]\n"
    "v_mfma_f32_16x16x32_bf16 v[140:143], a[236:239], a[68:71], v[140:143]\n"
    "v_mfma_f32_16x16x32_bf16 v[124:127], a[240:243], a[72:75], v[124:127]\n"
    "buffer_load_dwordx4 a[20:23], v29, s[48:51], s65 offen offset:1024 sc1\n"
    "v_mfma_f32_16x16x32_bf16 v[128:131], a[244:247], a[72:75], v[128:131]\n"
    "v_mfma_f32_16x16x32_bf16 v[132:135], a[248:251], a[72:75], v[132:135]\n"
    "s_waitcnt lgkmcnt(0)\n"
    "s_barrier\n"
    "ds_read_b128 v[32:35], v16 offset:0\n"
    "ds_read_b128 v[36:39], v17 offset:0\n"
    "v_mfma_f32_16x16x32_bf16 v[136:139], a[252:255], a[72:75], v[136:139]\n"
    "ds_read_b128 v[40:43], v16 offset:5120\n"
    "ds_read_b128 v[44:47], v17 offset:5120\n"
    "ds_read_b128 v[48:51], v16 offset:10240\n"
    "v_mfma_f32_16x16x32_bf16 v[140:143], v[160:163], a[72:75], v[140:143]\n"
    "buffer_load_dwordx4 a[24:27], v29, s[48:51], s65 offen offset:2048 sc1\n"
    "v_mfma_f32_16x16x32_bf16 v[124:127], v[164:167], a[76:79], v[124:127]\n"
    "ds_read_b128 v[52:55], v17 offset:10240\n"
    "ds_read_b128 v[56:59], v16 offset:15360\n"
    "ds_read_b128 v[60:63], v17 offset:15360\n"
    "v_mfma_f32_16x16x32_bf16 v[128:131], v[168:171], a[76:79], v[128:131]\n"
    "v_mfma_f32_16x16x32_bf16 v[132:135], v[172:175], a[76:79], v[132:135]\n"
    "v_mfma_f32_16x16x32_bf16 v[136:139], v[176:179], a[76:79], v[136:139]\n"
    "buffer_load_dwordx4 a[28:31], v29, s[48:51], s65 offen offset:3072 sc1\n"
    "v_mfma_f32_16x16x32_bf16 v[140:143], v[180:183], a[76:79], v[140:143]\n"
    "v_mfma_f32_16x16x32_bf16 v[124:127], v[184:187], a[80:83], v[124:127]\n"
    "s_waitcnt lgkmcnt(5)\n"
    "v_add_f32 v32, v32, v40\n"
    "v_add_f32 v33, v33, v41\n"
    "v_add_f32 v34, v34, v42\n"
    "v_mfma_f32_16x16x32_bf16 v[128:131], v[188:191], a[80:83], v[128:131]\n"
    "v_add_f32 v35, v35, v43\n"
    "s_waitcnt lgkmcnt(4)\n"
    "v_add_f32 v36, v36, v44\n"
    "v_add_f32 v37, v37, v45\n"
    "v_mfma_f32_16x16x32_bf16 v[132:135], v[192:195], a[80:83], v[132:135]\n"
    "s_add_u32 s65, s80, 0x6e000\n"
    "buffer_load_dwordx4 a[32:35], v29, s[48:51], s65 offen offset:0 sc1\n"
    "v_mfma_f32_16x16x32_bf16 v[136:139], v[196:199], a[80:83], v[136:139]\n"
    "v_add_f32 v38, v38, v46\n"
    "v_add_f32 v39, v39, v47\n"
    "s_waitcnt lgkmcnt(1)\n"
    "v_add_f32 v48, v48, v56\n"
    "v_mfma_f32_16x16x32_bf16 v[140:143], v[200:203], a[80:83], v[140:143]\n"
    "v_add_f32 v49, v49, v57\n"
    "v_add_f32 v50, v50, v58\n"
    "v_add_f32 v51, v51, v59\n"
    "s_waitcnt vmcnt(9)\n"
    "v_mfma_f32_16x16x32_bf16 v[124:127], v[204:207], a[84:87], v[124:127]\n"
    "s_waitcnt lgkmcnt(0)\n"
    "v_add_f32 v52, v52, v60\n"
    "v_add_f32 v53, v53, v61\n"
    "v_mfma_f32_16x16x32_bf16 v[128:131], v[208:211], a[84:87], v[128:131]\n"
    "s_add_u32 s82, s76, 98304\n"
    "buffer_load_dwordx2 v[156:157], v159, s[48:51], s82 offen offset:0 sc1\n"
    "v_mfma_f32_16x16x32_bf16 v[132:135], v[212:215], a[84:87], v[132:135]\n"
    "v_add_f32 v54, v54, v62\n"
    "v_add_f32 v55, v55, v63\n"
    "v_add_f32 v32, v32, v48\n"
    "v_mfma_f32_16x16x32_bf16 v[136:139], v[216:219], a[84:87], v[136:139]\n"
    "v_add_f32 v33, v33, v49\n"
    "v_cvt_pk_bf16_f32 v148, v32, v33\n"
    "v_add_f32 v34, v34, v50\n"
    "v_mfma_f32_16x16x32_bf16 v[140:143], v[220:223], a[84:87], v[140:143]\n"
    "v_add_f32 v35, v35, v51\n"
    "v_cvt_pk_bf16_f32 v149, v34, v35\n"
    "v_add_f32 v36, v36, v52\n"
    "v_mfma_f32_16x16x32_bf16 v[124:127], v[224:227], a[88:91], v[124:127]\n"
    "buffer_load_dwordx4 a[36:39], v29, s[48:51], s65 offen offset:1024 sc1\n"
    "v_mfma_f32_16x16x32_bf16 v[128:131], v[228:231], a[88:91], v[128:131]\n"
    "v_add_f32 v37, v37, v53\n"
    "v_cvt_pk_bf16_f32 v150, v36, v37\n"
    "v_add_f32 v38, v38, v54\n"
    "v_mfma_f32_16x16x32_bf16 v[132:135], v[232:235], a[88:91], v[132:135]\n"
    "v_add_f32 v39, v39, v55\n"
    "v_cvt_pk_bf16_f32 v151, v38, v39\n"
    "s_add_u32 s72, s66, 0x0\n"
    "v_mfma_f32_16x16x32_bf16 v[136:139], v[236:239], a[88:91], v[136:139]\n"
    "buffer_store_dwordx4 v[148:151], v18, s[52:55], s72 offen offset:0\n"
    "v_mfma_f32_16x16x32_bf16 v[140:143], v[240:243], a[88:91], v[140:143]\n"
    "buffer_load_dwordx4 a[40:43], v29, s[48:51], s65 offen offset:2048 sc1\n"
    "v_mfma_f32_16x16x32_bf16 v[124:127], v[244:247], a[92:95], v[124:127]\n"
    "v_mfma_f32_16x16x32_bf16 v[128:131], v[248:251], a[92:95], v[128:131]\n"
    "v_mfma_f32_16x16x32_bf16 v[132:135], v[252:255], a[92:95], v[132:135]\n"
    "v_mfma_f32_16x16x32_bf16 v[136:139], a[112:115], a[92:95], v[136:139]\n"
    "buffer_load_dwordx4 a[44:47], v29, s[48:51], s65 offen offset:3072 sc1\n"
    "v_mfma_f32_16x16x32_bf16 v[140:143], a[116:119], a[92:95], v[140:143]\n"
    "v_mfma_f32_16x16x32_bf16 v[124:127], a[120:123], a[96:99], v[124:127]\n"
    "v_mfma_f32_16x16x32_bf16 v[128:131], a[124:127], a[96:99], v[128:131]\n"
    "v_mfma_f32_16x16x32_bf16 v[132:135], a[128:131], a[96:99], v[132:135]\n"
    "s_add_u32 s65, s78, 0x6f000\n"
    "buffer_load_dwordx4 a[48:51], v29, s[48:51], s65 offen offset:0 sc1\n"
    "v_mfma_f32_16x16x32_bf16 v[136:139], a[132:135], a[96:99], v[136:139]\n"
    "v_mfma_f32_16x16x32_bf16 v[140:143], a[136:139], a[96:99], v[140:143]\n"
    "v_mfma_f32_16x16x32_bf16 v[124:127], a[140:143], a[100:103], v[124:127]\n"
    "v_mfma_f32_16x16x32_bf16 v[128:131], a[144:147], a[100:103], v[128:131]\n"
    "buffer_load_dwordx4 a[52:55], v29, s[48:51], s65 offen offset:1024 sc1\n"
    "v_mfma_f32_16x16x32_bf16 v[132:135], a[148:151], a[100:103], v[132:135]\n"
    "v_mfma_f32_16x16x32_bf16 v[136:139], a[152:155], a[100:103], v[136:139]\n"
    "v_mfma_f32_16x16x32_bf16 v[140:143], a[156:159], a[100:103], v[140:143]\n"
    "v_mfma_f32_16x16x32_bf16 v[124:127], v[64:67], a[104:107], v[124:127]\n"
    "v_mfma_f32_16x16x32_bf16 v[128:131], v[68:71], a[104:107], v[128:131]\n"
    "v_mfma_f32_16x16x32_bf16 v[132:135], v[72:75], a[104:107], v[132:135]\n"
    "v_mfma_f32_16x16x32_bf16 v[136:139], v[76:79], a[104:107], v[136:139]\n"
    "v_mfma_f32_16x16x32_bf16 v[140:143], v[80:83], a[104:107], v[140:143]\n"
    "v_mfma_f32_16x16x32_bf16 v[124:127], v[84:87], a[108:111], v[124:127]\n"
    "v_mfma_f32_16x16x32_bf16 v[128:131], v[88:91], a[108:111], v[128:131]\n"
    "v_mfma_f32_16x16x32_bf16 v[132:135], v[92:95], a[108:111], v[132:135]\n"
    "v_mfma_f32_16x16x32_bf16 v[136:139], v[96:99], a[108:111], v[136:139]\n"
    "v_mfma_f32_16x16x32_bf16 v[140:143], v[100:103], a[108:111], v[140:143]\n"
    "s_waitcnt vmcnt(6)\n"
    "v_cmp_eq_u64 vcc, s[56:57], v[156:157]\n"
    "s_and_b64 s[70:71], vcc, s[68:69]\n"
    "s_cmp_eq_u64 s[70:71], s[68:69]\n"
    "s_cbranch_scc1 L_ok7_%=\n"
    "s_mov_b32 s74, 0x4000\n"
    "L_spin7_%=:\n"
    "s_sleep 2\n"
    "s_add_u32 s82, s76, 98304\n"
    "buffer_load_dwordx2 v[156:157], v159, s[48:51], s82 offen sc1\n"
    "s_waitcnt vmcnt(0)\n"
    "v_cmp_eq_u64 vcc, s[56:57], v[156:157]\n"
    "s_and_b64 s[70:71], vcc, s[68:69]\n"
    "s_cmp_eq_u64 s[70:71], s[68:69]\n"
    "s_cbranch_scc1 L_ok7_%=\n"
    "s_sub_u32 s74, s74, 1\n"
    "s_cmp_lg_u32 s74, 0\n"
    "s_cbranch_scc1 L_spin7_%=\n"
    "L_ok7_%=:\n"
    "v_mfma_f32_16x16x32_bf16 v[104:107], a[160:163], a[0:3], 0\n"
    "s_add_u32 s65, s80, 0xa2000\n"
    "v_mfma_f32_16x16x32_bf16 v[108:111], a[164:167], a[0:3], 0\n"
    "buffer_load_dwordx4 a[56:59], v29, s[48:51], s65 offen offset:0 sc1\n"
    "v_mfma_f32_16x16x32_bf16 v[112:115], a[168:171], a[0:3], 0\n"
    "v_mfma_f32_16x16x32_bf16 v[116:119], a[172:175], a[0:3], 0\n"
    "v_mfma_f32_16x16x32_bf16 v[120:123], a[176:179], a[0:3], 0\n"
    "buffer_load_dwordx4 a[60:63], v29, s[48:51], s65 offen offset:1024 sc1\n"
    "v_mfma_f32_16x16x32_bf16 v[104:107], a[180:183], a[4:7], v[104:107]\n"
    "v_mfma_f32_16x16x32_bf16 v[108:111], a[184:187], a[4:7], v[108:111]\n"
    "v_mfma_f32_16x16x32_bf16 v[112:115], a[188:191], a[4:7], v[112:115]\n"
    "v_mfma_f32_16x16x32_bf16 v[116:119], a[192:195], a[4:7], v[116:119]\n"
    "buffer_load_dwordx4 a[64:67], v29, s[48:51], s65 offen offset:2048 sc1\n"
    "v_mfma_f32_16x16x32_bf16 v[120:123], a[196:199], a[4:7], v[120:123]\n"
    "ds_write_b128 v19, v[124:127] offset:20480\n"
    "v_mfma_f32_16x16x32_bf16 v[104:107], a[200:203], a[8:11], v[104:107]\n"
    "ds_write_b128 v19, v[128:131] offset:21504\n"
    "v_mfma_f32_16x16x32_bf16 v[108:111], a[204:207], a[8:11], v[108:111]\n"
    "ds_write_b128 v19, v[132:135] offset:22528\n"
    "v_mfma_f32_16x16x32_bf16 v[112:115], a[208:211], a[8:11], v[112:115]\n"
    "buffer_load_dwordx4 a[68:71], v29, s[48:51], s65 offen offset:3072 sc1\n"
    "v_mfma_f32_16x16x32_bf16 v[116:119], a[212:215], a[8:11], v[116:119]\n"
    "ds_write_b128 v19, v[136:139] offset:23552\n"
    "v_mfma_f32_16x16x32_bf16 v[120:123], a[216:219], a[8:11], v[120:123]\n"
    "ds_write_b128 v20, v[140:143] offset:24576\n"
    "v_mfma_f32_16x16x32_bf16 v[104:107], a[220:223], a[12:15], v[104:107]\n"
    "v_mfma_f32_16x16x32_bf16 v[108:111], a[224:227], a[12:15], v[108:111]\n"
    "s_add_u32 s65, s80, 0xa3000\n"
    "buffer_load_dwordx4 a[72:75], v29, s[48:51], s65 offen offset:0 sc1\n"
    "v_mfma_f32_16x16x32_bf16 v[112:115], a[228:231], a[12:15], v[112:115]\n"
    "v_mfma_f32_16x16x32_bf16 v[116:119], a[232:235], a[12:15], v[116:119]\n"
    "v_mfma_f32_16x16x32_bf16 v[120:123], a[236:239], a[12:15], v[120:123]\n"
    "v_mfma_f32_16x16x32_bf16 v[104:107], a[240:243], a[16:19], v[104:107]\n"
    "buffer_load_dwordx4 a[76:79], v29, s[48:51], s65 offen offset:1024 sc1\n"
    "v_mfma_f32_16x16x32_bf16 v[108:111], a[244:247], a[16:19], v[108:111]\n"
    "v_mfma_f32_16x16x32_bf16 v[112:115], a[248:251], a[16:19], v[112:115]\n"
    "s_waitcnt lgkmcnt(0)\n"
    "s_barrier\n"
    "ds_read_b128 v[32:35], v16 offset:20480\n"
    "ds_read_b128 v[36:39], v17 offset:20480\n"
    "v_mfma_f32_16x16x32_bf16 v[116:119], a[252:255], a[16:19], v[116:119]\n"
    "ds_read_b128 v[40:43], v16 offset:25600\n"
    "ds_read_b128 v[44:47], v17 offset:25600\n"
    "ds_read_b128 v[48:51], v16 offset:30720\n"
    "v_mfma_f32_16x16x32_bf16 v[120:123], v[160:163], a[16:19], v[120:123]\n"
    "buffer_load_dwordx4 a[80:83], v29, s[48:51], s65 offen offset:2048 sc1\n"
    "v_mfma_f32_16x16x32_bf16 v[104:107], v[164:167], a[20:23], v[104:107]\n"
    "ds_read_b128 v[52:55], v17 offset:30720\n"
    "ds_read_b128 v[56:59], v16 offset:35840\n"
    "ds_read_b128 v[60:63], v17 offset:35840\n"
    "v_mfma_f32_16x16x32_bf16 v[108:111], v[168:171], a[20:23], v[108:111]\n"
    "v_mfma_f32_16x16x32_bf16 v[112:115], v[172:175], a[20:23], v[112:115]\n"
    "v_mfma_f32_16x16x32_bf16 v[116:119], v[176:179], a[20:23], v[116:119]\n"
    "buffer_load_dwordx4 a[84:87], v29, s[48:51], s65 offen offset:3072 sc1\n"
    "v_mfma_f32_16x16x32_bf16 v[120:123], v[180:183], a[20:23], v[120:123]\n"
    "v_mfma_f32_16x16x32_bf16 v[104:107], v[184:187], a[24:27], v[104:107]\n"
    "s_waitcnt lgkmcnt(5)\n"
    "v_add_f32 v32, v32, v40\n"
    "v_add_f32 v33, v33, v41\n"
    "v_add_f32 v34, v34, v42\n"
    "v_mfma_f32_16x16x32_bf16 v[108:111], v[188:191], a[24:27], v[108:111]\n"
    "v_add_f32 v35, v35, v43\n"
    "s_waitcnt lgkmcnt(4)\n"
    "v_add_f32 v36, v36, v44\n"
    "v_add_f32 v37, v37, v45\n"
    "v_mfma_f32_16x16x32_bf16 v[112:115], v[192:195], a[24:27], v[112:115]\n"
    "s_add_u32 s65, s80, 0xa4000\n"
    "buffer_load_dwordx4 a[88:91], v29, s[48:51], s65 offen offset:0 sc1\n"
    "v_mfma_f32_16x16x32_bf16 v[116:119], v[196:199], a[24:27], v[116:119]\n"
    "v_add_f32 v38, v38, v46\n"
    "v_add_f32 v39, v39, v47\n"
    "s_waitcnt lgkmcnt(1)\n"
    "v_add_f32 v48, v48, v56\n"
    "v_mfma_f32_16x16x32_bf16 v[120:123], v[200:203], a[24:27], v[120:123]\n"
    "v_add_f32 v49, v49, v57\n"
    "v_add_f32 v50, v50, v58\n"
    "v_add_f32 v51, v51, v59\n"
    "s_waitcnt vmcnt(9)\n"
    "v_mfma_f32_16x16x32_bf16 v[104:107], v[204:207], a[28:31], v[104:107]\n"
    "s_waitcnt lgkmcnt(0)\n"
    "v_add_f32 v52, v52, v60\n"
    "v_add_f32 v53, v53, v61\n"
    "v_mfma_f32_16x16x32_bf16 v[108:111], v[208:211], a[28:31], v[108:111]\n"
    "buffer_load_dwordx4 a[92:95], v29, s[48:51], s65 offen offset:1024 sc1\n"
    "v_mfma_f32_16x16x32_bf16 v[112:115], v[212:215], a[28:31], v[112:115]\n"
    "v_add_f32 v54, v54, v62\n"
    "v_add_f32 v55, v55, v63\n"
    "v_add_f32 v32, v32, v48\n"
    "v_mfma_f32_16x16x32_bf16 v[116:119], v[216:219], a[28:31], v[116:119]\n"
    "v_add_f32 v33, v33, v49\n"
    "v_cvt_pk_bf16_f32 v152, v32, v33\n"
    "v_add_f32 v34, v34, v50\n"
    "v_mfma_f32_16x16x32_bf16 v[120:123], v[220:223], a[28:31], v[120:123]\n"
    "v_add_f32 v35, v35, v51\n"
    "v_cvt_pk_bf16_f32 v153, v34, v35\n"
    "v_add_f32 v36, v36, v52\n"
    "v_mfma_f32_16x16x32_bf16 v[104:107], v[224:227], a[32:35], v[104:107]\n"
    "buffer_load_dwordx4 a[96:99], v29, s[48:51], s65 offen offset:2048 sc1\n"
    "v_mfma_f32_16x16x32_bf16 v[108:111], v[228:231], a[32:35], v[108:111]\n"
    "v_add_f32 v37, v37, v53\n"
    "v_cvt_pk_bf16_f32 v154, v36, v37\n"
    "v_add_f32 v38, v38, v54\n"
    "v_mfma_f32_16x16x32_bf16 v[112:115], v[232:235], a[32:35], v[112:115]\n"
    "v_add_f32 v39, v39, v55\n"
    "v_cvt_pk_bf16_f32 v155, v38, v39\n"
    "s_add_u32 s72, s66, 0x14000\n"
    "v_mfma_f32_16x16x32_bf16 v[116:119], v[236:239], a[32:35], v[116:119]\n"
    "buffer_store_dwordx4 v[152:155], v18, s[52:55], s72 offen offset:0\n"
    "v_mfma_f32_16x16x32_bf16 v[120:123], v[240:243], a[32:35], v[120:123]\n"
    "buffer_load_dwordx4 a[100:103], v29, s[48:51], s65 offen offset:3072 sc1\n"
    "v_mfma_f32_16x16x32_bf16 v[104:107], v[244:247], a[36:39], v[104:107]\n"
    "v_mfma_f32_16x16x32_bf16 v[108:111], v[248:251], a[36:39], v[108:111]\n"
    "v_mfma_f32_16x16x32_bf16 v[112:115], v[252:255], a[36:39], v[112:115]\n"
    "v_mfma_f32_16x16x32_bf16 v[116:119], a[112:115], a[36:39], v[116:119]\n"
    "s_add_u32 s65, s78, 0xa5000\n"
    "buffer_load_dwordx4 a[104:107], v29, s[48:51], s65 offen offset:0 sc1\n"
    "v_mfma_f32_16x16x32_bf16 v[120:123], a[116:119], a[36:39], v[120:123]\n"
    "v_mfma_f32_16x16x32_bf16 v[104:107], a[120:123], a[40:43], v[104:107]\n"
    "v_mfma_f32_16x16x32_bf16 v[108:111], a[124:127], a[40:43], v[108:111]\n"
    "v_mfma_f32_16x16x32_bf16 v[112:115], a[128:131], a[40:43], v[112:115]\n"
    "buffer_load_dwordx4 a[108:111], v29, s[48:51], s65 offen offset:1024 sc1\n"
    "v_mfma_f32_16x16x32_bf16 v[116:119], a[132:135], a[40:43], v[116:119]\n"
    "v_mfma_f32_16x16x32_bf16 v[120:123], a[136:139], a[40:43], v[120:123]\n"
    "v_mfma_f32_16x16x32_bf16 v[104:107], a[140:143], a[44:47], v[104:107]\n"
    "v_mfma_f32_16x16x32_bf16 v[108:111], a[144:147], a[44:47], v[108:111]\n"
    "v_mfma_f32_16x16x32_bf16 v[112:115], a[148:151], a[44:47], v[112:115]\n"
    "v_mfma_f32_16x16x32_bf16 v[116:119], a[152:155], a[44:47], v[116:119]\n"
    "v_mfma_f32_16x16x32_bf16 v[120:123], a[156:159], a[44:47], v[120:123]\n"
    "v_mfma_f32_16x16x32_bf16 v[104:107], v[64:67], a[48:51], v[104:107]\n"
    "v_mfma_f32_16x16x32_bf16 v[108:111], v[68:71], a[48:51], v[108:111]\n"
    "v_mfma_f32_16x16x32_bf16 v[112:115], v[72:75], a[48:51], v[112:115]\n"
    "v_mfma_f32_16x16x32_bf16 v[116:119], v[76:79], a[48:51], v[116:119]\n"
    "v_mfma_f32_16x16x32_bf16 v[120:123], v[80:83], a[48:51], v[120:123]\n"
    "v_mfma_f32_16x16x32_bf16 v[104:107], v[84:87], a[52:55], v[104:107]\n"
    "v_mfma_f32_16x16x32_bf16 v[108:111], v[88:91], a[52:55], v[108:111]\n"
    "v_mfma_f32_16x16x32_bf16 v[112:115], v[92:95], a[52:55], v[112:115]\n"
    "v_mfma_f32_16x16x32_bf16 v[116:119], v[96:99], a[52:55], v[116:119]\n"
    "v_mfma_f32_16x16x32_bf16 v[120:123], v[100:103], a[52:55], v[120:123]\n"
    "s_waitcnt vmcnt(8)\n"
    "v_mfma_f32_16x16x32_bf16 v[124:127], a[160:163], a[56:59], 0\n"
    "v_mfma_f32_16x16x32_bf16 v[128:131], a[164:167], a[56:59], 0\n"
    "v_mfma_f32_16x16x32_bf16 v[132:135], a[168:171], a[56:59], 0\n"
    "v_mfma_f32_16x16x32_bf16 v[136:139], a[172:175], a[56:59], 0\n"
    "v_mfma_f32_16x16x32_bf16 v[140:143], a[176:179], a[56:59], 0\n"
    "v_mfma_f32_16x16x32_bf16 v[124:127], a[180:183], a[60:63], v[124:127]\n"
    "v_mfma_f32_16x16x32_bf16 v[128:131], a[184:187], a[60:63], v[128:131]\n"
    "v_mfma_f32_16x16x32_bf16 v[132:135], a[188:191], a[60:63], v[132:135]\n"
    "v_mfma_f32_16x16x32_bf16 v[136:139], a[192:195], a[60:63], v[136:139]\n"
    "v_mfma_f32_16x16x32_bf16 v[140:143], a[196:199], a[60:63], v[140:143]\n"
    "ds_write_b128 v19, v[104:107] offset:0\n"
    "v_mfma_f32_16x16x32_bf16 v[124:127], a[200:203], a[64:67], v[124:127]\n"
    "ds_write_b128 v19, v[108:111] offset:1024\n"
    "v_mfma_f32_16x16x32_bf16 v[128:131], a[204:207], a[64:67], v[128:131]\n"
    "ds_write_b128 v19, v[112:115] offset:2048\n"
    "v_mfma_f32_16x16x32_bf16 v[132:135], a[208:211], a[64:67], v[132:135]\n"
    "ds_write_b128 v19, v[116:119] offset:3072\n"
    "v_mfma_f32_16x16x32_bf16 v[136:139], a[212:215], a[64:67], v[136:139]\n"
    "ds_write_b128 v20, v[120:123] offset:4096\n"
    "v_mfma_f32_16x16x32_bf16 v[140:143], a[216:219], a[64:67], v[140:143]\n"
    "v_mfma_f32_16x16x32_bf16 v[124:127], a[220:223], a[68:71], v[124:127]\n"
    "v_mfma_f32_16x16x32_bf16 v[128:131], a[224:227], a[68:71], v[128:131]\n"
    "v_mfma_f32_16x16x32_bf16 v[132:135], a[228:231], a[68:71], v[132:135]\n"
    "v_mfma_f32_16x16x32_bf16 v[136:139], a[232:235], a[68:71], v[136:139]\n"
    "v_mfma_f32_16x16x32_bf16 v[140:143], a[236:239], a[68:71], v[140:143]\n"
    "v_mfma_f32_16x16x32_bf16 v[124:127], a[240:243], a[72:75], v[124:127]\n"
    "v_mfma_f32_16x16x32_bf16 v[128:131], a[244:247], a[72:75], v[128:131]\n"
    "v_mfma_f32_16x16x32_bf16 v[132:135], a[248:251], a[72:75], v[132:135]\n"
    "s_waitcnt lgkmcnt(0)\n"
    "s_barrier\n"
    "ds_read_b128 v[32:35], v16 offset:0\n"
    "ds_read_b128 v[36:39], v17 offset:0\n"
    "v_mfma_f32_16x16x32_bf16 v[136:139], a[252:255], a[72:75], v[136:139]\n"
    "ds_read_b128 v[40:43], v16 offset:5120\n"
    "ds_read_b128 v[44:47], v17 offset:5120\n"
    "ds_read_b128 v[48:51], v16 offset:10240\n"
    "v_mfma_f32_16x16x32_bf16 v[140:143], v[160:163], a[72:75], v[140:143]\n"
    "ds_read_b128 v[52:55], v17 offset:10240\n"
    "ds_read_b128 v[56:59], v16 offset:15360\n"
    "ds_read_b128 v[60:63], v17 offset:15360\n"
    "v_mfma_f32_16x16x32_bf16 v[124:127], v[164:167], a[76:79], v[124:127]\n"
    "v_mfma_f32_16x16x32_bf16 v[128:131], v[168:171], a[76:79], v[128:131]\n"
    "v_mfma_f32_16x16x32_bf16 v[132:135], v[172:175], a[76:79], v[132:135]\n"
    "v_mfma_f32_16x16x32_bf16 v[136:139], v[176:179], a[76:79], v[136:139]\n"
    "v_mfma_f32_16x16x32_bf16 v[140:143], v[180:183], a[76:79], v[140:143]\n"
    "v_mfma_f32_16x16x32_bf16 v[124:127], v[184:187], a[80:83], v[124:127]\n"
    "s_waitcnt lgkmcnt(5)\n"
    "v_add_f32 v32, v32, v40\n"
    "v_add_f32 v33, v33, v41\n"
    "v_add_f32 v34, v34, v42\n"
    "v_mfma_f32_16x16x32_bf16 v[128:131], v[188:191], a[80:83], v[128:131]\n"
    "v_add_f32 v35, v35, v43\n"
    "s_waitcnt lgkmcnt(4)\n"
    "v_add_f32 v36, v36, v44\n"
    "v_add_f32 v37, v37, v45\n"
    "v_mfma_f32_16x16x32_bf16 v[132:135], v[192:195], a[80:83], v[132:135]\n"
    "v_add_f32 v38, v38, v46\n"
    "v_add_f32 v39, v39, v47\n"
    "s_waitcnt lgkmcnt(1)\n"
    "v_add_f32 v48, v48, v56\n"
    "v_mfma_f32_16x16x32_bf16 v[136:139], v[196:199], a[80:83], v[136:139]\n"
    "v_add_f32 v49, v49, v57\n"
    "v_add_f32 v50, v50, v58\n"
    "v_add_f32 v51, v51, v59\n"
    "v_mfma_f32_16x16x32_bf16 v[140:143], v[200:203], a[80:83], v[140:143]\n"
    "s_waitcnt lgkmcnt(0)\n"
    "v_add_f32 v52, v52, v60\n"
    "v_add_f32 v53, v53, v61\n"
    "v_add_f32 v54, v54, v62\n"
    "s_waitcnt vmcnt(0)\n"
    "v_mfma_f32_16x16x32_bf16 v[124:127], v[204:207], a[84:87], v[124:127]\n"
    "v_add_f32 v55, v55, v63\n"
    "v_add_f32 v32, v32, v48\n"
    "v_mfma_f32_16x16x32_bf16 v[128:131], v[208:211], a[84:87], v[128:131]\n"
    "v_add_f32 v33, v33, v49\n"
    "v_cvt_pk_bf16_f32 v148, v32, v33\n"
    "v_add_f32 v34, v34, v50\n"
    "v_mfma_f32_16x16x32_bf16 v[132:135], v[212:215], a[84:87], v[132:135]\n"
    "v_add_f32 v35, v35, v51\n"
    "v_cvt_pk_bf16_f32 v149, v34, v35\n"
    "v_add_f32 v36, v36, v52\n"
    "v_mfma_f32_16x16x32_bf16 v[136:139], v[216:219], a[84:87], v[136:139]\n"
    "v_add_f32 v37, v37, v53\n"
    "v_cvt_pk_bf16_f32 v150, v36, v37\n"
    "v_add_f32 v38, v38, v54\n"
    "v_mfma_f32_16x16x32_bf16 v[140:143], v[220:223], a[84:87], v[140:143]\n"
    "v_add_f32 v39, v39, v55\n"
    "v_cvt_pk_bf16_f32 v151, v38, v39\n"
    "s_add_u32 s72, s66, 0x28000\n"
    "v_mfma_f32_16x16x32_bf16 v[124:127], v[224:227], a[88:91], v[124:127]\n"
    "buffer_store_dwordx4 v[148:151], v18, s[52:55], s72 offen offset:0\n"
    "v_mfma_f32_16x16x32_bf16 v[128:131], v[228:231], a[88:91], v[128:131]\n"
    "v_mfma_f32_16x16x32_bf16 v[132:135], v[232:235], a[88:91], v[132:135]\n"
    "v_mfma_f32_16x16x32_bf16 v[136:139], v[236:239], a[88:91], v[136:139]\n"
    "v_mfma_f32_16x16x32_bf16 v[140:143], v[240:243], a[88:91], v[140:143]\n"
    "v_mfma_f32_16x16x32_bf16 v[124:127], v[244:247], a[92:95], v[124:127]\n"
    "v_mfma_f32_16x16x32_bf16 v[128:131], v[248:251], a[92:95], v[128:131]\n"
    "v_mfma_f32_16x16x32_bf16 v[132:135], v[252:255], a[92:95], v[132:135]\n"
    "v_mfma_f32_16x16x32_bf16 v[136:139], a[112:115], a[92:95], v[136:139]\n"
    "v_mfma_f32_16x16x32_bf16 v[140:143], a[116:119], a[92:95], v[140:143]\n"
    "v_mfma_f32_16x16x32_bf16 v[124:127], a[120:123], a[96:99], v[124:127]\n"
    "v_mfma_f32_16x16x32_bf16 v[128:131], a[124:127], a[96:99], v[128:131]\n"
    "v_mfma_f32_16x16x32_bf16 v[132:135], a[128:131], a[96:99], v[132:135]\n"
    "v_mfma_f32_16x16x32_bf16 v[136:139], a[132:135], a[96:99], v[136:139]\n"
    "v_mfma_f32_16x16x32_bf16 v[140:143], a[136:139], a[96:99], v[140:143]\n"
    "v_mfma_f32_16x16x32_bf16 v[124:127], a[140:143], a[100:103], v[124:127]\n"
    "v_mfma_f32_16x16x32_bf16 v[128:131], a[144:147], a[100:103], v[128:131]\n"
    "v_mfma_f32_16x16x32_bf16 v[132:135], a[148:151], a[100:103], v[132:135]\n"
    "v_mfma_f32_16x16x32_bf16 v[136:139], a[152:155], a[100:103], v[136:139]\n"
    "v_mfma_f32_16x16x32_bf16 v[140:143], a[156:159], a[100:103], v[140:143]\n"
    "v_mfma_f32_16x16x32_bf16 v[124:127], v[64:67], a[104:107], v[124:127]\n"
    "v_mfma_f32_16x16x32_bf16 v[128:131], v[68:71], a[104:107], v[128:131]\n"
    "v_mfma_f32_16x16x32_bf16 v[132:135], v[72:75], a[104:107], v[132:135]\n"
    "v_mfma_f32_16x16x32_bf16 v[136:139], v[76:79], a[104:107], v[136:139]\n"
    "v_mfma_f32_16x16x32_bf16 v[140:143], v[80:83], a[104:107], v[140:143]\n"
    "v_mfma_f32_16x16x32_bf16 v[124:127], v[84:87], a[108:111], v[124:127]\n"
    "v_mfma_f32_16x16x32_bf16 v[128:131], v[88:91], a[108:111], v[128:131]\n"
    "v_mfma_f32_16x16x32_bf16 v[132:135], v[92:95], a[108:111], v[132:135]\n"
    "v_mfma_f32_16x16x32_bf16 v[136:139], v[96:99], a[108:111], v[136:139]\n"
    "v_mfma_f32_16x16x32_bf16 v[140:143], v[100:103], a[108:111], v[140:143]\n"
    "s_nop 3\n"
    "ds_write_b128 v19, v[124:127] offset:20480\n"
    "ds_write_b128 v19, v[128:131] offset:21504\n"
    "ds_write_b128 v19, v[132:135] offset:22528\n"
    "ds_write_b128 v19, v[136:139] offset:23552\n"
    "ds_write_b128 v20, v[140:143] offset:24576\n"
    "s_waitcnt lgkmcnt(0)\n"
    "s_barrier\n"
    "ds_read_b128 v[32:35], v16 offset:20480\n"
    "ds_read_b128 v[36:39], v17 offset:20480\n"
    "ds_read_b128 v[40:43], v16 offset:25600\n"
    "ds_read_b128 v[44:47], v17 offset:25600\n"
    "ds_read_b128 v[48:51], v16 offset:30720\n"
    "ds_read_b128 v[52:55], v17 offset:30720\n"
    "ds_read_b128 v[56:59], v16 offset:35840\n"
    "ds_read_b128 v[60:63], v17 offset:35840\n"
    "s_waitcnt lgkmcnt(5)\n"
    "v_add_f32 v32, v32, v40\n"
    "v_add_f32 v33, v33, v41\n"
    "v_add_f32 v34, v34, v42\n"
    "v_add_f32 v35, v35, v43\n"
    "s_waitcnt lgkmcnt(4)\n"
    "v_add_f32 v36, v36, v44\n"
    "v_add_f32 v37, v37, v45\n"
    "v_add_f32 v38, v38, v46\n"
    "v_add_f32 v39, v39, v47\n"
    "s_waitcnt lgkmcnt(1)\n"
    "v_add_f32 v48, v48, v56\n"
    "v_add_f32 v49, v49, v57\n"
    "v_add_f32 v50, v50, v58\n"
    "v_add_f32 v51, v51, v59\n"
    "s_waitcnt lgkmcnt(0)\n"
    "v_add_f32 v52, v52, v60\n"
    "v_add_f32 v53, v53, v61\n"
    "v_add_f32 v54, v54, v62\n"
    "v_add_f32 v55, v55, v63\n"
    "v_add_f32 v32, v32, v48\n"
    "v_add_f32 v33, v33, v49\n"
    "v_cvt_pk_bf16_f32 v152, v32, v33\n"
    "v_add_f32 v34, v34, v50\n"
    "v_add_f32 v35, v35, v51\n"
    "v_cvt_pk_bf16_f32 v153, v34, v35\n"
    "v_add_f32 v36, v36, v52\n"
    "v_add_f32 v37, v37, v53\n"
    "v_cvt_pk_bf16_f32 v154, v36, v37\n"
    "v_add_f32 v38, v38, v54\n"
    "v_add_f32 v39, v39, v55\n"
    "v_cvt_pk_bf16_f32 v155, v38, v39\n"
    "s_add_u32 s72, s66, 0x3c000\n"
    "buffer_store_dwordx4 v[152:155], v18, s[52:55], s72 offen offset:0\n"
    :
    : [ws] "s"(ws), [x_lo] "s"((unsigned)(size_t)x), [x_hi] "s"((unsigned)((size_t)x >> 32)), [wg] "s"(wg),
      [wu_lo] "s"((unsigned)(size_t)wu), [wu_hi] "s"((unsigned)((size_t)wu >> 32)), [out] "s"(out),
      [pk] "s"(pk), [tag_lo] "s"(tag_lo), [tag_hi] "s"(tag_hi)
    : "v0", "v1", "v2", "v3", "v4", "v5", "v6", "v7", "v8", "v9", "v10", "v11", "v12", "v13", "v14", "v15", "v16", "v17", "v18", "v19", "v20", "v21", "v22", "v23", "v24", "v25", "v26", "v27", "v28", "v29", "v30", "v31", "v32", "v33", "v34", "v35", "v36", "v37", "v38", "v39", "v40", "v41", "v42", "v43", "v44", "v45", "v46", "v47", "v48", "v49", "v50", "v51", "v52", "v53", "v54", "v55", "v56", "v57", "v58", "v59", "v60", "v61", "v62", "v63", "v64", "v65", "v66", "v67", "v68", "v69", "v70", "v71", "v72", "v73", "v74", "v75", "v76", "v77", "v78", "v79", "v80", "v81", "v82", "v83", "v84", "v85", "v86", "v87", "v88", "v89", "v90", "v91", "v92", "v93", "v94", "v95", "v96", "v97", "v98", "v99", "v100", "v101", "v102", "v103", "v104", "v105", "v106", "v107", "v108", "v109", "v110", "v111", "v112", "v113", "v114", "v115", "v116", "v117", "v118", "v119", "v120", "v121", "v122", "v123", "v124", "v125", "v126", "v127", "v128", "v129", "v130", "v131", "v132", "v133", "v134", "v135", "v136", "v137", "v138", "v139", "v140", "v141", "v142", "v143", "v144", "v145", "v146", "v147", "v148", "v149", "v150", "v151", "v152", "v153", "v154", "v155", "v156", "v157", "v158", "v159", "v160", "v161", "v162", "v163", "v164", "v165", "v166", "v167", "v168", "v169", "v170", "v171", "v172", "v173", "v174", "v175", "v176", "v177", "v178", "v179", "v180", "v181", "v182", "v183", "v184", "v185", "v186", "v187", "v188", "v189", "v190", "v191", "v192", "v193", "v194", "v195", "v196", "v197", "v198", "v199", "v200", "v201", "v202", "v203", "v204", "v205", "v206", "v207", "v208", "v209", "v210", "v211", "v212", "v213", "v214", "v215", "v216", "v217", "v218", "v219", "v220", "v221", "v222", "v223", "v224", "v225", "v226", "v227", "v228", "v229", "v230", "v231", "v232", "v233", "v234", "v235", "v236", "v237", "v238", "v239", "v240", "v241", "v242", "v243", "v244", "v245", "v246", "v247", "v248", "v249", "v250", "v251", "v252", "v253", "v254", "v255", "a0", "a1", "a2", "a3", "a4", "a5", "a6", "a7", "a8", "a9", "a10", "a11", "a12", "a13", "a14", "a15", "a16", "a17", "a18", "a19", "a20", "a21", "a22", "a23", "a24", "a25", "a26", "a27", "a28", "a29", "a30", "a31", "a32", "a33", "a34", "a35", "a36", "a37", "a38", "a39", "a40", "a41", "a42", "a43", "a44", "a45", "a46", "a47", "a48", "a49", "a50", "a51", "a52", "a53", "a54", "a55", "a56", "a57", "a58", "a59", "a60", "a61", "a62", "a63", "a64", "a65", "a66", "a67", "a68", "a69", "a70", "a71", "a72", "a73", "a74", "a75", "a76", "a77", "a78", "a79", "a80", "a81", "a82", "a83", "a84", "a85", "a86", "a87", "a88", "a89", "a90", "a91", "a92", "a93", "a94", "a95", "a96", "a97", "a98", "a99", "a100", "a101", "a102", "a103", "a104", "a105", "a106", "a107", "a108", "a109", "a110", "a111", "a112", "a113", "a114", "a115", "a116", "a117", "a118", "a119", "a120", "a121", "a122", "a123", "a124", "a125", "a126", "a127", "a128", "a129", "a130", "a131", "a132", "a133", "a134", "a135", "a136", "a137", "a138", "a139", "a140", "a141", "a142", "a143", "a144", "a145", "a146", "a147", "a148", "a149", "a150", "a151", "a152", "a153", "a154", "a155", "a156", "a157", "a158", "a159", "a160", "a161", "a162", "a163", "a164", "a165", "a166", "a167", "a168", "a169", "a170", "a171", "a172", "a173", "a174", "a175", "a176", "a177", "a178", "a179", "a180", "a181", "a182", "a183", "a184", "a185", "a186", "a187", "a188", "a189", "a190", "a191", "a192", "a193", "a194", "a195", "a196", "a197", "a198", "a199", "a200", "a201", "a202", "a203", "a204", "a205", "a206", "a207", "a208", "a209", "a210", "a211", "a212", "a213", "a214", "a215", "a216", "a217", "a218", "a219", "a220", "a221", "a222", "a223", "a224", "a225", "a226", "a227", "a228", "a229", "a230", "a231", "a232", "a233", "a234", "a235", "a236", "a237", "a238", "a239", "a240", "a241", "a242", "a243", "a244", "a245", "a246", "a247", "a248", "a249", "a250", "a251", "a252", "a253", "a254", "a255", "s34", "s35", "s36", "s37", "s38", "s39", "s40", "s41", "s42", "s43", "s44", "s45", "s46", "s47", "s48", "s49", "s50", "s51", "s52", "s53", "s54", "s55", "s56", "s57", "s58", "s59", "s60", "s61", "s62", "s63", "s64", "s65", "s66", "s67", "s68", "s69", "s70", "s71", "s72", "s73", "s74", "s75", "s76", "s77", "s78", "s79", "s80", "s81", "s82", "s83", "s84", "s85", "s86", "s87", "s88", "s89", "s90", "s91", "s92", "s93", "s94", "s95", "s96", "s97", "s98", "s99", "vcc", "scc", "memory");
}

extern "C" void kernel_launch(void* const* d_in, const int* in_sizes, int n_in, void* d_out, int out_size, void* d_ws, size_t ws_size,
                              hipStream_t stream) {
    geglu_fused<<<256, 256, 0, stream>>>((const char*)d_in[0], (const char*)d_in[1], (const char*)d_in[2], (const char*)d_in[3], (char*)d_out, (char*)d_ws);
}
```
